```python
import torch

def _softmax(x, axis=-1):
    return torch.softmax(x, dim=axis)
def _gelu(x):
    return torch.nn.functional.gelu(x, approximate="tanh")
def _silu(x):
    return torch.nn.functional.silu(x)
def _sigmoid(x):
    return torch.sigmoid(x)
def _layernorm(x, axis=-1, eps=1e-5):
    m = x.mean(dim=axis, keepdim=True)
    v = x.var(dim=axis, keepdim=True, unbiased=False)
    return (x - m) / torch.sqrt(v + eps)
def _scatter_add(tgt, idx, src, axis=0):
    return tgt.clone().index_add_(axis, idx, src)
def _segment_sum(src, seg, n):
    out = torch.zeros((n,) + src.shape[1:], dtype=src.dtype)
    return out.index_add_(0, seg, src)
def _pad(x, pw):
    flat = tuple(v for pair in reversed(pw) for v in pair)
    return torch.nn.functional.pad(x, flat)

kernel_name = 'gated_mix__sliding_block__moe_arch_mod__prompt'

def setup_inputs():

    _w = (lambda a: (lambda t: t.to(torch.bfloat16) if t.is_floating_point() else t)(torch.as_tensor(a)))
    def _A_setup():
        B, S, D = 1, 2048, 576
        return {
            "x": _w((lambda *s: torch.randn(*s, dtype=torch.bfloat16) * 0.05)(B, S, D)),
            "wqkv": _w((lambda *s: torch.randn(*s, dtype=torch.bfloat16) * 0.8)(D, 3 * D)),
            "wo": _w((lambda *s: torch.randn(*s, dtype=torch.bfloat16) * 0.05)(D, D)),
        }
    _A_in = _A_setup()

    def _B_setup():
        B, S, D, F, C = 1, 2048, 576, 4608, 1
        return {
            "x": _w((lambda *s: torch.randn(*s, dtype=torch.bfloat16) * 0.05)(B, S, D)),
            "w_r": _w((lambda *s: torch.randn(*s, dtype=torch.bfloat16) * 3.2)(D)),
            "w_gate": _w((lambda *s: torch.randn(*s, dtype=torch.bfloat16) * 0.2)(D, F)),
            "w_up": _w((lambda *s: torch.randn(*s, dtype=torch.bfloat16) * 0.2)(D, F)),
            "w_down": _w((lambda *s: torch.randn(*s, dtype=torch.bfloat16) * 0.2)(F, D)),
        }
    _B_in = _B_setup()

    return {
        "A_x": _A_in["x"],
        "A_wqkv": _A_in["wqkv"],
        "A_wo": _A_in["wo"],
        "B_x": _B_in["x"],
        "B_w_r": _B_in["w_r"],
        "B_w_gate": _B_in["w_gate"],
        "B_w_up": _B_in["w_up"],
        "B_w_down": _B_in["w_down"],
        "gate_w": _w((lambda *s: torch.randn(*s, dtype=torch.bfloat16) * 0.05)(576) * 16.0),
    }

def reference(A_x, A_wqkv, A_wo, B_x, B_w_r, B_w_gate, B_w_up, B_w_down, gate_w):
    def _A_ref(x, wqkv, wo):
        B, S, D = x.shape
        H = 32; Dh = D // H; W = 256
        qkv = (lambda x, *d: x.permute(*d))((x @ wqkv).reshape(B, S, 3, H, Dh), 2, 0, 3, 1, 4)
        q, k, v = qkv[0], qkv[1], qkv[2]
        scores = torch.einsum("bhqd,bhkd->bhqk", q, k) * (Dh ** -0.5)
        idx = torch.arange(S)
        mask = (idx[:, None] - idx[None, :] >= 0) & (idx[:, None] - idx[None, :] < W)
        scores = torch.where(mask[None, None], scores, -1e9)
        p = _softmax(scores, axis=-1)
        o = torch.einsum("bhqk,bhkd->bhqd", p, v)
        return (lambda x, *d: x.permute(*d))(o, 0, 2, 1, 3).reshape(B, S, D) @ wo

    def _B_ref(x, w_r, w_gate, w_up, w_down):
        B, S, D = x.shape
        C = 1
        r = x @ w_r
        idx = torch.argsort(-r, axis=-1)[:, :C]
        idx_e = (lambda x, n, axis: torch.repeat_interleave(x, n, dim=axis))(idx.reshape(B, C, 1), D, axis=2)
        x_sel = (lambda x, i, axis: torch.take_along_dim(x, i, dim=axis))(x, idx_e, axis=1)
        g = _silu(x_sel @ w_gate)
        u = x_sel @ w_up
        y = (g * u) @ w_down
        r_sel = _sigmoid((lambda x, i, axis: torch.take_along_dim(x, i, dim=axis))(r, idx, axis=1))
        y = y * r_sel.reshape(B, C, 1)
        b_off = (torch.arange(B) * S).reshape(B, 1)
        flat_idx = (idx + b_off).reshape(B * C)
        zero = (x * 0.0).reshape(B * S, D)
        upd = _scatter_add(zero, flat_idx, y.reshape(B * C, D), axis=0)
        return x + upd.reshape(B, S, D)

    x = A_x
    g = _sigmoid((x * gate_w).sum(-1, keepdims=True))
    a = _A_ref(x, A_wqkv, A_wo)
    b = _B_ref(x, B_w_r, B_w_gate, B_w_up, B_w_down)
    return g * a + (1 - g) * b
```

```cpp
#include <hip/hip_runtime.h>
#include <hip/hip_bf16.h>
typedef unsigned short u16;
__device__ inline float b2f(u16 v){ return __uint_as_float(((unsigned)v)<<16); }
__device__ inline u16 f2b(float f){ unsigned u=__float_as_uint(f); u += 0x7FFF + ((u>>16)&1); return (u16)(u>>16); }
constexpr int S=2048, D=576, F=4608, H=32, DH=18, W=256;

__global__ void k_qkv(const u16* x, const u16* w, u16* qkv){
  __shared__ float xs[16][D];
  int r0 = blockIdx.y*16; int c = blockIdx.x*256+threadIdx.x;
  for(int i=threadIdx.x;i<16*D;i+=256) xs[i/D][i%D]=b2f(x[(r0+i/D)*D+i%D]);
  __syncthreads();
  if(c>=3*D) return;
  float acc[16]; for(int i=0;i<16;i++) acc[i]=0;
  for(int k=0;k<D;k++){ float wv=b2f(w[k*3*D+c]);
#pragma unroll
    for(int i=0;i<16;i++) acc[i]+=xs[i][k]*wv; }
  for(int i=0;i<16;i++) qkv[(r0+i)*3*D+c]=f2b(acc[i]);
}
__global__ void k_rg(const u16* x, const u16* wr, const u16* gw, float* r, float* g){
  int row = blockIdx.x*4 + (threadIdx.x>>6); int lane=threadIdx.x&63;
  float a=0,b=0;
  for(int k=lane;k<D;k+=64){ float xv=b2f(x[row*D+k]); a+=xv*b2f(wr[k]); b+=xv*b2f(gw[k]); }
  for(int m=32;m>=1;m>>=1){ a+=__shfl_xor(a,m); b+=__shfl_xor(b,m);}
  if(lane==0){ r[row]=a; g[row]=1.f/(1.f+__expf(-b)); }
}
__global__ void k_attn(const u16* qkv, u16* o){
  int q = blockIdx.x*64+threadIdx.x; int h=blockIdx.y;
  float qv[DH]; for(int d=0;d<DH;d++) qv[d]=b2f(qkv[q*3*D+h*DH+d])*0.23570226f;
  float m=-1e30f,l=0,acc[DH]; for(int d=0;d<DH;d++) acc[d]=0;
  int k0=q-W+1; if(k0<0)k0=0;
  for(int k=k0;k<=q;k++){ float s=0; for(int d=0;d<DH;d++) s+=qv[d]*b2f(qkv[k*3*D+D+h*DH+d]);
    float mn=fmaxf(m,s); float a=__expf(m-mn), p=__expf(s-mn); l=l*a+p; for(int d=0;d<DH;d++) acc[d]=acc[d]*a+p*b2f(qkv[k*3*D+2*D+h*DH+d]); m=mn; }
  for(int d=0;d<DH;d++) o[q*D+h*DH+d]=f2b(acc[d]/l);
}
__global__ void k_moe1(const u16* x, const float* r, const u16* wg, const u16* wu, float* hbuf, int* idxo){
  __shared__ float sv[256]; __shared__ int si[256];
  float bv=-1e30f; int bi=0; for(int i=threadIdx.x;i<S;i+=256){ if(r[i]>bv){bv=r[i];bi=i;} }
  sv[threadIdx.x]=bv; si[threadIdx.x]=bi; __syncthreads();
  for(int s=128;s>=1;s>>=1){ if(threadIdx.x<s){ if(sv[threadIdx.x+s]>sv[threadIdx.x]){sv[threadIdx.x]=sv[threadIdx.x+s]; si[threadIdx.x]=si[threadIdx.x+s];}} __syncthreads(); }
  int idx=si[0]; if(blockIdx.x==0&&threadIdx.x==0) *idxo=idx;
  int f=blockIdx.x*256+threadIdx.x; float a=0,b=0;
  for(int k=0;k<D;k++){ float xv=b2f(x[idx*D+k]); a+=xv*b2f(wg[k*F+f]); b+=xv*b2f(wu[k*F+f]); }
  a=b2f(f2b(a)); b=b2f(f2b(b)); float g=b2f(f2b(a/(1.f+__expf(-a)))); hbuf[f]=b2f(f2b(g*b));
}
__global__ void k_moe2(const float* hbuf, const u16* wd, float* y){
  int d=blockIdx.x*64+(threadIdx.x&63); int p=threadIdx.x>>6; float a=0;
  for(int f=p;f<F;f+=16) a+=hbuf[f]*b2f(wd[f*D+d]);
  __shared__ float s[16][64]; s[p][threadIdx.x&63]=a; __syncthreads();
  if(p==0){ float t=0; for(int i=0;i<16;i++) t+=s[i][threadIdx.x]; y[d]=t; }
}
__global__ void k_out(const u16* x, const u16* o, const u16* wo, const float* r, const float* g, const float* y, const int* idxp, u16* out){
  __shared__ float os[16][D];
  int r0=blockIdx.y*16; int c=blockIdx.x*192+threadIdx.x;
  for(int i=threadIdx.x;i<16*D;i+=192) os[i/D][i%D]=b2f(o[(r0+i/D)*D+i%D]);
  __syncthreads();
  float acc[16]; for(int i=0;i<16;i++) acc[i]=0;
  for(int k=0;k<D;k++){ float wv=b2f(wo[k*D+c]);
#pragma unroll
    for(int i=0;i<16;i++) acc[i]+=os[i][k]*wv; }
  int idx=*idxp;
  for(int i=0;i<16;i++){ int row=r0+i; float gg=g[row]; float b=b2f(x[row*D+c]);
    if(row==idx){ float rs=b2f(f2b(r[row])); b+=y[c]*(1.f/(1.f+__expf(-rs))); }
    out[row*D+c]=f2b(gg*acc[i]+(1.f-gg)*b); }
}
extern "C" void kernel_launch(void* const* d_in, const int* in_sizes, int n_in,
                              void* d_out, int out_size, void* d_ws, size_t ws_size,
                              hipStream_t stream) {
  const u16* x=(const u16*)d_in[0]; const u16* wqkv=(const u16*)d_in[1]; const u16* wo=(const u16*)d_in[2];
  const u16* wr=(const u16*)d_in[4]; const u16* wg=(const u16*)d_in[5]; const u16* wu=(const u16*)d_in[6]; const u16* wd=(const u16*)d_in[7]; const u16* gw=(const u16*)d_in[8];
  char* ws=(char*)d_ws;
  u16* qkv=(u16*)ws; u16* o=(u16*)(ws+(8<<20)); float* r=(float*)(ws+(12<<20)); float* g=r+S; float* hb=g+S; float* y=hb+F; int* idx=(int*)(y+D);
  k_qkv<<<dim3(7,S/16),256,0,stream>>>(x,wqkv,qkv);
  k_rg<<<S/4,256,0,stream>>>(x,wr,gw,r,g);
  k_attn<<<dim3(S/64,H),64,0,stream>>>(qkv,o);
  k_moe1<<<F/256,256,0,stream>>>(x,r,wg,wu,hb,idx);
  k_moe2<<<D/64,1024,0,stream>>>(hb,wd,y);
  k_out<<<dim3(3,S/16),192,0,stream>>>(x,o,wo,r,g,y,idx,(u16*)d_out);
}
```

```cpp
#include <hip/hip_runtime.h>
#include <hip/hip_bf16.h>
typedef unsigned short u16;
__device__ inline float b2f(u16 v){ return __uint_as_float(((unsigned)v)<<16); }
__device__ inline u16 f2b(float f){ unsigned u=__float_as_uint(f); u += 0x7FFF + ((u>>16)&1); return (u16)(u>>16); }
constexpr int S=2048, D=576, F=4608, H=32, DH=18, W=256;

__global__ void k_qkv(const u16* x, const u16* w, u16* qkv){
  __shared__ float xs[16][D];
  int r0 = blockIdx.y*16; int c = blockIdx.x*256+threadIdx.x;
  for(int i=threadIdx.x;i<16*D;i+=256) xs[i/D][i%D]=b2f(x[(r0+i/D)*D+i%D]);
  __syncthreads();
  if(c>=3*D) return;
  float acc[16]; for(int i=0;i<16;i++) acc[i]=0;
  for(int k=0;k<D;k++){ float wv=b2f(w[k*3*D+c]);
#pragma unroll
    for(int i=0;i<16;i++) acc[i]+=xs[i][k]*wv; }
  for(int i=0;i<16;i++) qkv[(r0+i)*3*D+c]=f2b(acc[i]);
}
__global__ void k_rg(const u16* x, const u16* wr, const u16* gw, float* r, float* g){
  int row = blockIdx.x*4 + (threadIdx.x>>6); int lane=threadIdx.x&63;
  float a=0,b=0;
  for(int k=lane;k<D;k+=64){ float xv=b2f(x[row*D+k]); a+=xv*b2f(wr[k]); b+=xv*b2f(gw[k]); }
  for(int m=32;m>=1;m>>=1){ a+=__shfl_xor(a,m); b+=__shfl_xor(b,m);}
  if(lane==0){ r[row]=a; g[row]=1.f/(1.f+__expf(-b)); }
}
__global__ void k_attn(const u16* qkv, u16* o){
  int q = blockIdx.x*64+threadIdx.x; int h=blockIdx.y;
  float qv[DH]; for(int d=0;d<DH;d++) qv[d]=b2f(qkv[q*3*D+h*DH+d])*0.23570226f;
  float m=-1e30f,l=0,acc[DH]; for(int d=0;d<DH;d++) acc[d]=0;
  int k0=q-W+1; if(k0<0)k0=0;
  for(int k=k0;k<=q;k++){ float s=0; for(int d=0;d<DH;d++) s+=qv[d]*b2f(qkv[k*3*D+D+h*DH+d]);
    float mn=fmaxf(m,s); float a=__expf(m-mn), p=__expf(s-mn); l=l*a+p; for(int d=0;d<DH;d++) acc[d]=acc[d]*a+p*b2f(qkv[k*3*D+2*D+h*DH+d]); m=mn; }
  for(int d=0;d<DH;d++) o[q*D+h*DH+d]=f2b(acc[d]/l);
}
__global__ void k_moe1(const u16* x, const float* r, const u16* wg, const u16* wu, float* hbuf, int* idxo){
  __shared__ float sv[256]; __shared__ int si[256];
  float bv=-1e30f; int bi=0; for(int i=threadIdx.x;i<S;i+=256){ if(r[i]>bv){bv=r[i];bi=i;} }
  sv[threadIdx.x]=bv; si[threadIdx.x]=bi; __syncthreads();
  for(int s=128;s>=1;s>>=1){ if(threadIdx.x<s){ if(sv[threadIdx.x+s]>sv[threadIdx.x]){sv[threadIdx.x]=sv[threadIdx.x+s]; si[threadIdx.x]=si[threadIdx.x+s];}} __syncthreads(); }
  int idx=si[0]; if(blockIdx.x==0&&threadIdx.x==0) *idxo=idx;
  int f=blockIdx.x*256+threadIdx.x; float a=0,b=0;
  for(int k=0;k<D;k++){ float xv=b2f(x[idx*D+k]); a+=xv*b2f(wg[k*F+f]); b+=xv*b2f(wu[k*F+f]); }
  a=b2f(f2b(a)); b=b2f(f2b(b)); float g=b2f(f2b(a/(1.f+__expf(-a)))); hbuf[f]=b2f(f2b(g*b));
}
__global__ void k_moe2(const float* hbuf, const u16* wd, float* y){
  int d=blockIdx.x*64+(threadIdx.x&63); int p=threadIdx.x>>6; float a=0;
  for(int f=p;f<F;f+=16) a+=hbuf[f]*b2f(wd[f*D+d]);
  __shared__ float s[16][64]; s[p][threadIdx.x&63]=a; __syncthreads();
  if(p==0){ float t=0; for(int i=0;i<16;i++) t+=s[i][threadIdx.x]; y[d]=t; }
}
__global__ void k_out(const u16* x, const u16* o, const u16* wo, const float* r, const float* g, const float* y, const int* idxp, u16* out){
  __shared__ float os[16][D];
  int r0=blockIdx.y*16; int c=blockIdx.x*192+threadIdx.x;
  for(int i=threadIdx.x;i<16*D;i+=192) os[i/D][i%D]=b2f(o[(r0+i/D)*D+i%D]);
  __syncthreads();
  float acc[16]; for(int i=0;i<16;i++) acc[i]=0;
  for(int k=0;k<D;k++){ float wv=b2f(wo[k*D+c]);
#pragma unroll
    for(int i=0;i<16;i++) acc[i]+=os[i][k]*wv; }
  int idx=*idxp;
  for(int i=0;i<16;i++){ int row=r0+i; float gg=g[row]; float b=b2f(x[row*D+c]);
    if(row==idx){ float rs=b2f(f2b(r[row])); b+=y[c]*(1.f/(1.f+__expf(-rs))); }
    out[row*D+c]=f2b(gg*acc[i]+(1.f-gg)*b); }
}

typedef unsigned v4u __attribute__((ext_vector_type(4)));
#define GAS __attribute__((address_space(1)))
#define R4(X) X X X X
#define R16(X) R4(R4(X))
#define R64(X) R4(R16(X))
#define R256(X) R4(R64(X))
#define U "v_max3_f32 v8,v16,v17,v18\n v_max3_f32 v9,v17,v18,v19\n v_max3_f32 v10,v18,v19,v20\n v_max3_f32 v11,v19,v20,v21\n"
#define CLOB "v8","v9","v10","v11","v16","v17","v18","v19","v20","v21"
__device__ inline void spin_report(unsigned long long cyc){
  unsigned long long r0=__builtin_amdgcn_s_memrealtime(); unsigned long long tgt=cyc*10; if(tgt>400000) tgt=400000;
  while(__builtin_amdgcn_s_memrealtime()-r0<tgt) __builtin_amdgcn_s_sleep(8);
}
template<int MODE>
__global__ void __launch_bounds__(512,2) probe(const char* a,const char* b,const char* c,const char* x, unsigned* sink){
  int lane=threadIdx.x&63; int w=__builtin_amdgcn_readfirstlane(threadIdx.x>>6);
  unsigned long long t0,t1;
  asm volatile("s_memtime %0\n\ts_waitcnt lgkmcnt(0)" : "=s"(t0) :: "memory");
  unsigned acc=0;
  if(MODE==0){   }
  if(MODE==1||MODE==2){
    v4u v[6]; size_t off=(size_t)blockIdx.x*20480+w*2048+lane*16;
    const char* ps[3]={a,b,c};
#pragma unroll
    for(int i=0;i<6;i++){ const GAS v4u* q=(const GAS v4u*)(ps[i>>1]+off+(i&1)*1024); v[i]= MODE==2? __builtin_nontemporal_load(q): *q; }
#pragma unroll
    for(int i=0;i<6;i++) acc^=v[i].x^v[i].y^v[i].z^v[i].w;
  }
  if(MODE==3){
    v4u v=*(const GAS v4u*)(x+(size_t)blockIdx.x*8192+w*1024+lane*16); acc=v.x^v.y^v.z^v.w;
  }
  if(MODE==4){ asm volatile(R4(R256(U)) ::: CLOB); }
  if(MODE==5){
    v4u v=*(const GAS v4u*)(c+(size_t)blockIdx.x*8192+w*1024+lane*16); acc=v.x^v.y^v.z^v.w;
  }
  asm volatile("":"+v"(acc));
  __syncthreads();
  asm volatile("s_memtime %0\n\ts_waitcnt lgkmcnt(0)" : "=s"(t1) :: "memory");
  if(acc==0x12345678) sink[0]=acc;
  unsigned long long d=t1-t0; if(MODE==4) d = d>20000? d-20000:0;
  spin_report(d);
}

extern "C" void kernel_launch(void* const* d_in, const int* in_sizes, int n_in,
                              void* d_out, int out_size, void* d_ws, size_t ws_size,
                              hipStream_t stream) {
  const u16* x=(const u16*)d_in[0]; const u16* wqkv=(const u16*)d_in[1]; const u16* wo=(const u16*)d_in[2];
  const u16* wr=(const u16*)d_in[4]; const u16* wg=(const u16*)d_in[5]; const u16* wu=(const u16*)d_in[6]; const u16* wd=(const u16*)d_in[7]; const u16* gw=(const u16*)d_in[8];
  char* ws=(char*)d_ws;
  u16* qkv=(u16*)ws; u16* o=(u16*)(ws+(8<<20)); float* r=(float*)(ws+(12<<20)); float* g=r+S; float* hb=g+S; float* y=hb+F; int* idx=(int*)(y+D);
  probe<0><<<256,512,0,stream>>>((const char*)wg,(const char*)wu,(const char*)wd,(const char*)x,(unsigned*)(ws+(40<<20)));
  k_qkv<<<dim3(7,S/16),256,0,stream>>>(x,wqkv,qkv);
  k_rg<<<S/4,256,0,stream>>>(x,wr,gw,r,g);
  k_attn<<<dim3(S/64,H),64,0,stream>>>(qkv,o);
  k_moe1<<<F/256,256,0,stream>>>(x,r,wg,wu,hb,idx);
  k_moe2<<<D/64,1024,0,stream>>>(hb,wd,y);
  k_out<<<dim3(3,S/16),192,0,stream>>>(x,o,wo,r,g,y,idx,(u16*)d_out);
}
```

```cpp
#include <hip/hip_runtime.h>

typedef unsigned short u16;
typedef unsigned int u32;
typedef unsigned long long u64;
typedef __attribute__((ext_vector_type(8))) __bf16 bf16x8;
typedef __attribute__((ext_vector_type(2))) __bf16 bf16x2;
typedef __attribute__((ext_vector_type(4))) short s16x4;
typedef __attribute__((ext_vector_type(2))) u32 u32x2;
typedef __attribute__((ext_vector_type(4))) u32 u32x4;
typedef __attribute__((ext_vector_type(2))) float f32x2;
typedef __attribute__((ext_vector_type(4))) float f32x4;
typedef __attribute__((ext_vector_type(16))) float f32x16;
#define GAS __attribute__((address_space(1)))
#define LAS __attribute__((address_space(3)))
#define CAS __attribute__((address_space(4)))
#define DI __device__ __forceinline__
#define RLX_AGENT __ATOMIC_RELAXED, __HIP_MEMORY_SCOPE_AGENT

constexpr u32 WS_EPOCH = 0;
constexpr u32 WS_RG = 256;
constexpr u32 WS_YF = 1024;
constexpr u32 WS_QF = 4096;
constexpr u32 WS_OF = 32768;
constexpr u32 WS_G = 65536;
constexpr u32 WS_HP = 81920;
constexpr u32 WS_YP = 393216;
constexpr u32 WS_QKV = 1u << 20;
constexpr u32 WS_O = 9u << 20;
constexpr u32 WS_OP = 11u << 20;
constexpr u32 WS_END = 12u << 20;
constexpr u32 WS_DBG = 16u << 20;

constexpr u32 L_GU = 32768;
constexpr u32 L_WO = 0;
constexpr u32 L_D1 = 82944;
constexpr u32 L_XS = 95232;
constexpr u32 L_H = 96384;
constexpr u32 L_SEL = 96448;
constexpr u32 L_RED8 = 96512;
constexpr u32 L_ONE = 97024;
constexpr u32 L_Q = 98304;
constexpr u32 PSTR = 3104;
constexpr u32 L_K = 107520;
constexpr u32 L_V = 135456;
constexpr u32 L_RED = 98304;
constexpr u32 L_YR = 141312;
constexpr u32 L_TOTAL = 163840;

constexpr float QSCALE = 0.34004648445f;
constexpr int SPIN_CAP = 1 << 16;

DI void dma(u32 voff, const void* sbase, u32 dst) {
    asm volatile("s_mov_b32 m0, %2\n\ts_nop 0\n\tglobal_load_lds_dwordx4 %0, %1" ::"v"(voff), "s"(sbase), "s"(dst) : "memory");
}
DI void dma_sc1(u32 voff, const void* sbase, u32 dst) {
    asm volatile("s_mov_b32 m0, %2\n\ts_nop 0\n\tglobal_load_lds_dwordx4 %0, %1 sc1" ::"v"(voff), "s"(sbase), "s"(dst) : "memory");
}
DI void dma4(u32 voff, const void* sbase, u32 dst) {
    asm volatile("s_mov_b32 m0, %2\n\ts_nop 0\n\tglobal_load_lds_dword %0, %1" ::"v"(voff), "s"(sbase), "s"(dst) : "memory");
}
#define WAITV(n) asm volatile("s_waitcnt vmcnt(" #n ")" ::: "memory")
#define BAR() asm volatile("s_waitcnt lgkmcnt(0)\n\ts_barrier" ::: "memory")
DI u32x4 ld128(u32 a) { return *(const LAS u32x4*)a; }
DI u32x2 ld64(u32 a) { return *(const LAS u32x2*)a; }
DI u32x2 ldtr(u32 a) { return __builtin_bit_cast(u32x2, __builtin_amdgcn_ds_read_tr16_b64_v4i16((LAS s16x4*)a)); }
DI bf16x8 mk8(u32x2 a, u32x2 b) { return __builtin_bit_cast(bf16x8, (u32x4){a.x, a.y, b.x, b.y}); }
DI bf16x8 as8(u32x4 a) { return __builtin_bit_cast(bf16x8, a); }
DI u32 pk2(float a, float b) { return __builtin_bit_cast(u32, __builtin_convertvector((f32x2){a, b}, bf16x2)); }
DI float bfr(float a) { return __uint_as_float(pk2(a, 0.f) << 16); }
DI float lo16(u32 v) { return __uint_as_float(v << 16); }
DI float hi16(u32 v) { return __uint_as_float(v & 0xffff0000u); }
DI float sigm(float v) { return __builtin_amdgcn_rcpf(1.f + __builtin_amdgcn_exp2f(-1.4426950408889634f * v)); }
#define MFMA16(a, b, c) __builtin_amdgcn_mfma_f32_16x16x32_bf16(a, b, c, 0, 0, 0)
#define MFMA32(a, b, c) __builtin_amdgcn_mfma_f32_32x32x16_bf16(a, b, c, 0, 0, 0)
DI int uni(int v) { return __builtin_amdgcn_readfirstlane(v); }

#define STAMP(n)

__global__ void __launch_bounds__(512, 2)
fused_kernel(const char* __restrict__ x, const char* __restrict__ wqkv, const char* __restrict__ wo, const char* __restrict__ wr,
             const char* __restrict__ wg, const char* __restrict__ wu, const char* __restrict__ wd, const char* __restrict__ gw,
             char* __restrict__ out, char* ws) {
    extern __shared__ __attribute__((aligned(16))) char smem[];
    const int tid = threadIdx.x, l = tid & 63, w = uni(tid >> 6);
    const int b = blockIdx.x, xcd = b & 7, slot = b >> 3;
    STAMP(0);
    u64 E;
    asm volatile("s_load_dwordx2 %0, %1, 0x0\n\ts_waitcnt lgkmcnt(0)" : "=s"(E) : "s"(ws) : "memory");
    const u64 TAG = E * 6364136223846793005ull + 1442695040888963407ull;
    const u32 tag32 = (u32)TAG;
    const __amdgpu_buffer_rsrc_t wsr = __builtin_amdgcn_make_buffer_rsrc(ws, 0, WS_END, 0x00020000);
    const int kq = l >> 4, q4 = (l & 15) >> 2, p4 = l & 3;
    const bool moe1 = slot >= 5;
    const int m1 = xcd * 27 + slot - 5, unit = m1 / 3, third = m1 - unit * 3;
    const u32 gu_off = (l >> 3) * 9216 + ((((l & 7) >> 1) ^ (((l >> 4) & 1) | ((w & 1) << 1))) * 32) + (l & 1) * 16;
    const u32 gu_base = (third * 192 + w * 8) * 9216 + unit * 128;

    if (slot < 28) {
        const int rl = slot / 7, cl = slot - rl * 7;
        const int rt = (xcd >> 1) * 4 + rl, ct = (xcd & 1) * 7 + cl;
        int s = (cl + 2 * rl + (xcd >> 1) + 4 * (xcd & 1)) % 9;
        const u32 xo0 = (rt * 128 + 16 * w + (l >> 3)) * 1152 + (((l & 7) ^ (l >> 4)) * 16);
        const u32 xo1 = (xo0 ^ 64) + 8 * 1152;
        u32 wch = (l & 15) ^ (2 * (kq | ((w & 1) << 2)));
        if (ct == 13) wch &= 7;
        const u32 wo0 = (8 * w + kq) * 3456 + ct * 256 + wch * 16;
        const u32 wo1 = wo0 + 4 * 3456;
        auto stage = [&](int st_, u32 dst) {
            const char* xb = x + st_ * 128;
            const char* wb = wqkv + st_ * (64 * 3456);
            dma(xo0, xb, dst + w * 2048);
            dma(wo0, wb, dst + 16384 + w * 2048);
            dma(xo1, xb, dst + w * 2048 + 1024);
            dma(wo1, wb, dst + 16384 + w * 2048 + 1024);
        };
#pragma unroll
        for (int i = 0; i < 3; i++) { stage(s, i * 32768); s = (s == 8) ? 0 : s + 1; }
        const int tg = w & 3, cg = w >> 2;
        const bool active = !(ct == 13 && cg == 1);
        const u32 xa0 = (32 * tg + (l & 15)) * 128 + ((kq ^ ((l & 15) >> 1)) * 16);
        const u32 gsw = q4 | ((kq & 1) << 2);
        u32 wa[4];
#pragma unroll
        for (int c = 0; c < 4; c++) wa[c] = 16384 + (8 * kq + q4) * 256 + (((4 * cg + c) ^ gsw) * 32) + p4 * 8;
        f32x4 acc[2][4];
#pragma unroll
        for (int a = 0; a < 2; a++)
#pragma unroll
            for (int c = 0; c < 4; c++) acc[a][c] = (f32x4){0.f, 0.f, 0.f, 0.f};
#pragma nounroll
        for (int i = 0; i < 9; i++) {
            if (i < 7) WAITV(8);
            else if (moe1) { if (i == 7) WAITV(8); else WAITV(6); }
            else { if (i == 7) WAITV(4); else WAITV(0); }
            BAR();
            if (i < 6) { stage(s, ((i + 3) & 3) * 32768); s = (s == 8) ? 0 : s + 1; }
            else if (moe1) {
                if (i == 6) {
                    dma(gu_off, wg + gu_base, L_GU + w * 1024);
                    dma(gu_off, wg + gu_base + 64 * 9216, L_GU + (w + 8) * 1024);
                    dma(gu_off, wg + gu_base + 128 * 9216, L_GU + (w + 16) * 1024);
                    dma(gu_off, wu + gu_base, L_GU + (w + 24) * 1024);
                } else if (i == 7) {
                    dma(gu_off, wu + gu_base + 64 * 9216, L_GU + (w + 32) * 1024);
                    dma(gu_off, wu + gu_base + 128 * 9216, L_GU + (w + 40) * 1024);
                }
            }
            if (active) {
                const u32 so = (i & 3) * 32768;
                bf16x8 xf[2][2], wf[2][4];
#pragma unroll
                for (int ks = 0; ks < 2; ks++) {
                    const u32 xa = (xa0 ^ (ks * 64)) + so;
                    xf[ks][0] = as8(ld128(xa)); xf[ks][1] = as8(ld128(xa + 2048));
#pragma unroll
                    for (int c = 0; c < 4; c++) {
                        const u32 a = wa[c] + so + ks * 8192;
                        wf[ks][c] = mk8(ldtr(a), ldtr(a + 1024));
                    }
                }
                __builtin_amdgcn_sched_barrier(0);
#pragma unroll
                for (int ks = 0; ks < 2; ks++)
#pragma unroll
                    for (int c = 0; c < 4; c++) {
                        acc[0][c] = MFMA16(wf[ks][c], xf[ks][0], acc[0][c]);
                        acc[1][c] = MFMA16(wf[ks][c], xf[ks][1], acc[1][c]);
                    }
            }
        }
        STAMP(1);
        if (active) {
            const int pb = ct * 32 + cg * 16;
            const float sc = pb < 144 ? QSCALE : 1.f;
            const u32 so = WS_QKV + (pb + kq) * 16384 + (rt * 128 + 32 * tg) * 8 + (l & 15) * 16;
#pragma unroll
            for (int c = 0; c < 4; c++) {
                u32x4 v = {pk2(acc[0][c][0] * sc, acc[0][c][1] * sc), pk2(acc[0][c][2] * sc, acc[0][c][3] * sc),
                           pk2(acc[1][c][0] * sc, acc[1][c][1] * sc), pk2(acc[1][c][2] * sc, acc[1][c][3] * sc)};
                __builtin_amdgcn_raw_buffer_store_b128(v, wsr, so + c * 65536, 0, 16);
            }
        }
        WAITV(0);
        BAR();
        if (tid == 448) __hip_atomic_store((GAS u64*)(ws + WS_QF + (rt * 14 + ct) * 128), TAG, RLX_AGENT);
    } else {
        const int row0 = xcd * 256 + (slot - 28) * 64;
        const GAS char* xp = (const GAS char*)x + (row0 + 8 * w + (l >> 3)) * 1152 + (l & 7) * 16;
        u32x4 xv[9];
#pragma unroll
        for (int i = 0; i < 9; i++) xv[i] = *(const GAS u32x4*)(xp + i * 128);
        float ra = 0.f, ga = 0.f;
#pragma unroll
        for (int i = 0; i < 9; i++) {
            const u32x4 a = *(const GAS u32x4*)((const GAS char*)wr + i * 128 + (l & 7) * 16);
            const u32x4 g = *(const GAS u32x4*)((const GAS char*)gw + i * 128 + (l & 7) * 16);
#pragma unroll
            for (int e = 0; e < 4; e++) {
                const u32 xe = xv[i][e], ae = a[e], ge = g[e];
                ra = __builtin_amdgcn_fdot2_f32_bf16(__builtin_bit_cast(bf16x2, xe), __builtin_bit_cast(bf16x2, ae), ra, false);
                ga = __builtin_amdgcn_fdot2_f32_bf16(__builtin_bit_cast(bf16x2, xe), __builtin_bit_cast(bf16x2, ge), ga, false);
            }
        }
#pragma unroll
        for (int m = 1; m < 8; m <<= 1) { ra += __shfl_xor(ra, m); ga += __shfl_xor(ga, m); }
        u32 rb = __float_as_uint(ra);
        rb = (rb & ~0x7ffu) | ((rb & 0x7ffu) ? 0x800u : 0u);
        u32 key = (rb & 0x80000000u) ? ~rb : (rb | 0x80000000u);
        key = (key & ~0x7ffu) | (2047u - (u32)(row0 + 8 * w + (l >> 3)));
#pragma unroll
        for (int m = 8; m < 64; m <<= 1) key = max(key, (u32)__shfl_xor((int)key, m));
        if ((l & 7) == 0) *(LAS float*)(L_RED8 + (8 * w + (l >> 3)) * 4) = sigm(ga);
        if (l == 0) *(LAS u32*)(L_RED8 + 256 + w * 4) = key;
        BAR();
        if (w == 0) {
            const u32 gv = *(const LAS u32*)(L_RED8 + l * 4);
            __builtin_amdgcn_raw_buffer_store_b32(gv, wsr, WS_G + (row0 + l) * 4, 0, 16);
            u32 k = *(const LAS u32*)(L_RED8 + 256 + (l & 7) * 4);
#pragma unroll
            for (int m = 1; m < 8; m <<= 1) k = max(k, (u32)__shfl_xor((int)k, m));
            WAITV(0);
            if (l == 0) __hip_atomic_store((GAS u64*)(ws + WS_RG + (xcd * 4 + slot - 28) * 8), ((u64)tag32 << 32) | k, RLX_AGENT);
        }
        STAMP(1);
#pragma unroll
        for (int e = 0; e < 6; e++) dma(gu_off, (e < 3 ? wg : wu) + gu_base + (e % 3) * (64 * 9216), L_GU + (w + 8 * e) * 1024);
        WAITV(0);
        BAR();
    }
    STAMP(2);

    const int ai = slot >> 1, aj = xcd * 2 + (slot & 1);
    if (w == 0) {
        u64 v = 0;
#pragma nounroll
        for (int sp = 0; sp < SPIN_CAP; sp++) {
            v = __hip_atomic_load((GAS u64*)(ws + WS_RG + (l & 31) * 8), RLX_AGENT);
            if (__all((u32)(v >> 32) == tag32)) break;
            __builtin_amdgcn_s_sleep(1);
        }
        u32 k = (u32)v;
#pragma unroll
        for (int m = 1; m < 32; m <<= 1) k = max(k, (u32)__shfl_xor((int)k, m));
        k = uni(k);
        const int idx = 2047 - (int)(k & 2047u);
        const u32 kb = k & ~0x7ffu;
        const u32 rb = (kb & 0x80000000u) ? (kb & 0x7fffffffu) : (~kb & ~0x7ffu);
        dma(l * 16, x + idx * 1152, L_XS);
        if (l < 8) dma(l * 16, x + idx * 1152 + 1024, L_XS + 1024);
        if (l == 0) *(LAS u32x2*)L_SEL = (u32x2){(u32)idx, __float_as_uint(bfr(sigm(bfr(__uint_as_float(rb)))))};
        WAITV(0);
    } else if (w == 1) {
        const int ty = l / 6, rr = (l - ty * 6) >> 1;
        const int prt = max(ai - 2 + (ty == 0 ? 2 : rr), 0);
        const int pc = min(ty, 2) * 144 + 9 * aj + ((l & 1) ? 8 : 0);
        const GAS u64* f = (const GAS u64*)(ws + WS_QF + (prt * 14 + (pc >> 5)) * 128);
#pragma nounroll
        for (int sp = 0; sp < SPIN_CAP; sp++) {
            u64 v = TAG;
            if (l < 18) v = __hip_atomic_load(f, RLX_AGENT);
            if (__all(v == TAG)) break;
            __builtin_amdgcn_s_sleep(1);
        }
    } else if (w == 2) {
        if (l < 32) *(LAS u32x2*)(L_ONE + l * 8) = (u32x2){0x00003f80u, 0u};
    }
    BAR();
    STAMP(3);
#pragma unroll
    for (int e = 0; e < 8; e++) {
        const int u = min(w + 8 * e, 62);
        int pc, prt;
        u32 dst;
        if (u < 9) { pc = u; prt = ai; dst = L_Q + u * 1024; }
        else {
            const int v = u - 9, rr = v / 18, z = v - rr * 18;
            pc = (z < 9) ? 144 + z : 288 + z - 9;
            prt = max(ai - 2 + rr, 0);
            dst = L_K + z * PSTR + rr * 1024;
        }
        dma_sc1(l * 16, ws + WS_QKV + (pc + 9 * aj) * 16384 + prt * 1024, dst);
    }
    if (moe1) {
        const int m = w >> 2;
        const u32 a0 = L_GU + m * 24576 + (8 * kq + q4) * 128 + (((w & 3) ^ ((q4 >> 1) | ((kq & 1) << 1))) * 32) + p4 * 8;
        const u32 xs = L_XS + (third * 192 + 8 * kq) * 2;
        f32x4 d = {0.f, 0.f, 0.f, 0.f};
#pragma unroll
        for (int ks = 0; ks < 6; ks++) d = MFMA16(as8(ld128(xs + ks * 64)), mk8(ldtr(a0 + ks * 4096), ldtr(a0 + ks * 4096 + 512)), d);
        if (l < 16)
            __hip_atomic_store((GAS u64*)(ws + WS_HP + ((m1 * 2 + m) * 64 + (w & 3) * 16 + l) * 8), ((u64)tag32 << 32) | __float_as_uint(d[0]), RLX_AGENT);
    }
    BAR();
    const int oc = slot & 7, orow = xcd * 256 + (slot >> 3) * 64;
#pragma unroll
    for (int e = 0; e < 12; e++) {
        const int u = w + 8 * e;
        if (e < 10 || (e == 10 && w == 0)) {
            const u32 id = u * 64 + l, row = (id * 7282u) >> 16;
            dma(row * 1008 + id * 16, wo + oc * 144, L_WO + u * 1024);
        } else {
            const int z = min(u - 81, 11);
            dma(l * 16, wd + b * 20736 + z * 1024, L_D1 + z * 1024);
        }
    }
    const int hh = w >> 2, t = hh ? 3 - (w & 3) : (w & 3), h = l >> 5, pos = l & 31, pm = hh ? 5 : 0;
    f32x16 mdiag, mold;
    {
        const float tq = (float)((pos >> 1) + 16 * (pos & 1)) + 0.5f - (float)(2 * h);
#pragma unroll
        for (int r = 0; r < 16; r++) {
            const float uu = (tq - (float)(((r & 3) >> 1) + 4 * (r >> 2) + 16 * (r & 1))) * 1e30f;
            mdiag[r] = fminf(uu, 0.f);
            mold[r] = fminf(-uu, 0.f);
        }
    }
    WAITV(12);
    BAR();
    STAMP(4);
    bf16x8 qb1;
    u32x4 qb2;
    {
        const u32 qa = L_Q + (32 * t + pos) * 8;
        qb1 = mk8(ld64(qa + (pm + 2 * h) * 1024), ld64(qa + (pm + 2 * h + 1) * 1024));
        const u32x2 z = ld64(qa + 4096);
        qb2 = (u32x4){(h | hh) ? 0u : z.x, (h == 0 && hh) ? z.y : 0u, 0u, h ? 0u : 0xff7f0000u};
    }
    BAR();
    dma(l * 16, wd + b * 20736 + 11520 + w * 1024, L_Q + w * 1024);
    if (w == 0) dma(l * 16, wd + b * 20736 + 11520 + 8192, L_Q + 8192);

    f32x16 o;
    {
        const u32 ka = L_K + (pm + 2 * h) * PSTR + (32 * t + pos) * 8;
        const u32 kb = L_K + 4 * PSTR + (32 * t + pos) * 8;
        const bool cst = (l & 16) && p4;
        const u32 va = cst ? L_ONE + (4 * h + q4) * 8 : L_V + ((l & 16) ? 4 : pm + p4) * PSTR + (32 * t + 4 * h + q4) * 8;
        const u32 vstep = cst ? 0u : 256u;
        const int nfirst = 8 - 4 * ai - t;
        const f32x16 zero = {0.f, 0.f, 0.f, 0.f, 0.f, 0.f, 0.f, 0.f, 0.f, 0.f, 0.f, 0.f, 0.f, 0.f, 0.f, 0.f};
        auto scores = [&](int n, const f32x16& c) {
            const bf16x8 k1 = mk8(ld64(ka + n * 256), ld64(ka + n * 256 + PSTR));
            const u32x2 z = ld64(kb + n * 256);
            const bf16x8 k2 = as8((u32x4){z.x, z.y, 0x3f803f80u, n < nfirst ? 0x3f803f80u : 0x00003f80u});
            return MFMA32(k2, as8(qb2), MFMA32(k1, qb1, c));
        };
#pragma nounroll
        for (int attempt = 0; attempt < 2; attempt++) {
            o = zero;
            f32x16 sc = scores(0, mold);
#pragma unroll
            for (int n = 0; n < 9; n++) {
                f32x16 sn;
                if (n < 8) sn = scores(n + 1, n == 7 ? mdiag : zero);
                const u32 v0 = va + n * vstep;
                const bf16x8 vf0 = mk8(ldtr(v0), ldtr(v0 + 64)), vf1 = mk8(ldtr(v0 + 128), ldtr(v0 + 192));
                u32 p[8];
#pragma unroll
                for (int r = 0; r < 8; r++) p[r] = pk2(__builtin_amdgcn_exp2f(sc[2 * r]), __builtin_amdgcn_exp2f(sc[2 * r + 1]));
                o = MFMA32(vf0, as8((u32x4){p[0], p[1], p[2], p[3]}), o);
                o = MFMA32(vf1, as8((u32x4){p[4], p[5], p[6], p[7]}), o);
                if (n < 8) sc = sn;
            }
            if (attempt) break;
            const float lsum = h ? o[8] : o[12];
            float mx = 0.f;
#pragma unroll
            for (int r = 0; r < 12; r++) mx = fmaxf(mx, fabsf(o[r]));
            const bool ok = lsum >= 0x1p-40f && lsum <= 0x1p60f && mx <= 0x1p120f;
            if (__all(ok)) break;
            float m = -3e38f;
#pragma nounroll
            for (int n = 0; n < 9; n++) {
                f32x16 c = zero;
                if (n == 0) c = mold;
                if (n == 8) c = mdiag;
                const f32x16 s2 = scores(n, c);
#pragma unroll
                for (int r = 0; r < 16; r++) m = fmaxf(m, s2[r]);
            }
            m = fmaxf(m, __shfl_xor(m, 32));
            const u32 mh = __float_as_uint(-m) & 0xffff0000u;
            const float r1 = -m - __uint_as_float(mh);
            const u32 mm = __float_as_uint(r1) & 0xffff0000u;
            const u32 ml = pk2(r1 - __uint_as_float(mm), 0.f) & 0xffffu;
            if (h == 0) { qb2.z = (mh >> 16) | mm; qb2.w = 0xff7f0000u | ml; }
        }
        const float li = __builtin_amdgcn_rcpf(h ? o[8] : o[12]);
        const u32 prow = 128 * ai + 32 * t + pos;
        const u32x4 v = {pk2(o[0] * li, o[1] * li), pk2(o[2] * li, o[3] * li), pk2(o[4] * li, o[5] * li), pk2(o[6] * li, o[7] * li)};
        __builtin_amdgcn_raw_buffer_store_b128(v, wsr, WS_O + ((4 * aj + 2 * hh + h) * 2048 + prow) * 16, 0, 16);
        const u32 z = hh ? pk2(o[10] * li, o[11] * li) : pk2(o[8] * li, o[9] * li);
        if (h == 0) __builtin_amdgcn_raw_buffer_store_b32(z, wsr, WS_OP + ((2 * aj + hh) * 2048 + prow) * 4, 0, 16);
    }
    STAMP(5);
    WAITV(0);
    BAR();
    if (tid == 448) __hip_atomic_store((GAS u64*)(ws + WS_OF + (ai * 16 + aj) * 128), TAG, RLX_AGENT);
    STAMP(6);

    if (w == 0) {
        const int ff = 18 * b + min(l, 17);
        const GAS u64* hp = (const GAS u64*)(ws + WS_HP + ((ff >> 6) * 384 + (ff & 63)) * 8);
        u64 v[6];
#pragma nounroll
        for (int sp = 0; sp < SPIN_CAP; sp++) {
            bool ok = true;
#pragma unroll
            for (int e = 0; e < 6; e++) { v[e] = __hip_atomic_load(hp + e * 64, RLX_AGENT); ok &= (u32)(v[e] >> 32) == tag32; }
            if (__all(ok)) break;
            __builtin_amdgcn_s_sleep(1);
        }
        const float gg = bfr(__uint_as_float((u32)v[0]) + __uint_as_float((u32)v[2]) + __uint_as_float((u32)v[4]));
        const float uu = bfr(__uint_as_float((u32)v[1]) + __uint_as_float((u32)v[3]) + __uint_as_float((u32)v[5]));
        const u32 hv = pk2(bfr(gg * sigm(gg)) * uu, 0.f);
        if (l < 32) *(LAS u16*)(L_H + l * 2) = l < 18 ? (u16)hv : (u16)0;
    }
    BAR();
    {
        const bf16x8 hb = as8(ld128(L_H + kq * 16));
        u32 ra[2];
#pragma unroll
        for (int z = 0; z < 2; z++) {
            const int k = 8 * kq + 4 * z + q4;
            ra[z] = (k < 10 ? L_D1 + k * 1152 : k < 18 ? L_Q + (k - 10) * 1152 : L_D1) + p4 * 8 + w * 32;
        }
#pragma unroll
        for (int e = 0; e < 5; e++) {
            if (e < 4 || w < 4) {
                const f32x4 d = MFMA16(mk8(ldtr(ra[0] + e * 256), ldtr(ra[1] + e * 256)), hb, ((f32x4){0.f, 0.f, 0.f, 0.f}));
                if ((l & 15) == 0) *(LAS u32x2*)(L_XS + (w + 8 * e) * 32 + kq * 8) = (u32x2){pk2(d[0], d[1]), pk2(d[2], d[3])};
            }
        }
    }
    BAR();
    if (w == 0) {
#pragma unroll
        for (int z = 0; z < 2; z++) {
            const u32 id = l + 64 * z, c = (id * 7282u) >> 16;
            if (id < 72) __builtin_amdgcn_raw_buffer_store_b128(ld128(L_XS + id * 16), wsr, WS_YP + c * (36864 - 144) + b * 144 + id * 16, 0, 16);
        }
        WAITV(0);
        if (l == 0) __hip_atomic_store((GAS u64*)(ws + WS_YF + b * 8), TAG, RLX_AGENT);
    } else if (w == 1) {
        const GAS u64* f = (const GAS u64*)(ws + WS_OF + ((orow >> 7) * 16 + (l & 15)) * 128);
#pragma nounroll
        for (int sp = 0; sp < SPIN_CAP; sp++) {
            u64 v = TAG;
            if (l < 16) v = __hip_atomic_load(f, RLX_AGENT);
            if (__all(v == TAG)) break;
            __builtin_amdgcn_s_sleep(1);
        }
    }
    u32x4 xm[2];
    float gm[2];
    u32 tok[2], rho[2], chn[2];
#pragma unroll
    for (int z = 0; z < 2; z++) {
        const u32 id = min(tid + 512 * z, 575);
        rho[z] = (id * 7282u) >> 16;
        chn[z] = id - rho[z] * 9;
        tok[z] = orow + (rho[z] & 32) + ((rho[z] & 31) >> 1) + 16 * (rho[z] & 1);
        if (z == 0 || w == 0) {
            xm[z] = *(const GAS u32x4*)((const GAS char*)x + tok[z] * 1152 + oc * 144 + chn[z] * 16);
            gm[z] = __uint_as_float(__builtin_amdgcn_raw_buffer_load_b32(wsr, WS_G + tok[z] * 4, 0, 16));
        }
    }
    BAR();
    STAMP(7);

    {
        const int tt = w & 3, kh = w >> 2;
        const u32 prow = orow + 16 * tt + (l & 15);
        u32x4 ob[9];
        const u32 oa = WS_O + ((36 * kh + kq) * 2048 + prow) * 16;
#pragma unroll
        for (int n = 0; n < 7; n++) ob[n] = __builtin_amdgcn_raw_buffer_load_b128(wsr, oa, n * 131072, 16);
        if (kh == 0) {
#pragma unroll
            for (int n = 7; n < 9; n++) ob[n] = __builtin_amdgcn_raw_buffer_load_b128(wsr, oa, n * 131072, 16);
        } else {
#pragma unroll
            for (int n = 7; n < 9; n++)
#pragma unroll
                for (int e = 0; e < 4; e++)
                    ob[n][e] = __builtin_amdgcn_raw_buffer_load_b32(wsr, WS_OP + (4 * kq * 2048 + prow) * 4, (16 * (n - 7) + e) * 8192, 16);
        }
        const u32 am = L_WO + (324 * kh + 20 * (kq >> 1) + 4 * (kq & 1) + q4) * 144 + p4 * 8;
        const u32 ap = L_WO + (72 * kq + 16 + q4) * 144 + p4 * 8;
        f32x4 acc[5];
#pragma unroll
        for (int c = 0; c < 5; c++) acc[c] = (f32x4){0.f, 0.f, 0.f, 0.f};
#pragma unroll
        for (int n = 0; n < 9; n++) {
            const bool tail = n >= 7 && kh;
            const u32 a0 = tail ? ap + (n - 7) * (288 * 144) : am + n * (36 * 144);
            const u32 a1 = a0 + (tail ? 36 * 144 : 8 * 144);
#pragma unroll
            for (int c = 0; c < 5; c++) acc[c] = MFMA16(mk8(ldtr(a0 + c * 32), ldtr(a1 + c * 32)), as8(ob[n]), acc[c]);
        }
#pragma unroll
        for (int c = 0; c < 5; c++) *(LAS f32x4*)(L_RED + kh * 21504 + (16 * tt + (l & 15)) * 336 + c * 64 + kq * 16) = acc[c];
    }
    BAR();
    STAMP(8);
    const u32x2 sel = ld64(L_SEL);
    const int idx = uni(sel.x);
    const bool fin = (idx >> 6) == (orow >> 6);
    if (fin) {
        if (w == 0) {
#pragma nounroll
        for (int sp = 0; sp < SPIN_CAP; sp++) {
                bool ok = true;
#pragma unroll
                for (int e = 0; e < 4; e++) ok &= __hip_atomic_load((GAS u64*)(ws + WS_YF + (l + 64 * e) * 8), RLX_AGENT) == TAG;
                if (__all(ok)) break;
                __builtin_amdgcn_s_sleep(1);
            }
            if (l == 0 && oc == 0) __hip_atomic_store((GAS u64*)(ws + WS_EPOCH), TAG, RLX_AGENT);
        }
        BAR();
#pragma unroll
        for (int e = 0; e < 5; e++)
            if (e < 4 || w < 4) dma_sc1(l * 16, ws + WS_YP + oc * 36864 + (w + 8 * e) * 1024, L_WO + (w + 8 * e) * 1024);
        WAITV(0);
        BAR();
        const u32 a0 = L_WO + (32 * w + 8 * kq + q4) * 144 + p4 * 8;
        const bf16x8 ones = as8((u32x4){0x3f803f80u, 0x3f803f80u, 0x3f803f80u, 0x3f803f80u});
#pragma unroll
        for (int c = 0; c < 5; c++) {
            const f32x4 d = MFMA16(mk8(ldtr(a0 + c * 32), ldtr(a0 + 4 * 144 + c * 32)), ones, ((f32x4){0.f, 0.f, 0.f, 0.f}));
            if ((l & 15) == 0) *(LAS f32x4*)(L_YR + w * 320 + c * 64 + kq * 16) = d;
        }
        BAR();
    }
#pragma unroll
    for (int z = 0; z < 2; z++) {
        if (z == 0 || w == 0) {
            const u32 a = L_RED + rho[z] * 336 + chn[z] * 32;
            const f32x4 a0 = *(const LAS f32x4*)a + *(const LAS f32x4*)(a + 21504);
            const f32x4 a1 = *(const LAS f32x4*)(a + 16) + *(const LAS f32x4*)(a + 21504 + 16);
            float bv[8];
#pragma unroll
            for (int e = 0; e < 4; e++) { bv[2 * e] = lo16(xm[z][e]); bv[2 * e + 1] = hi16(xm[z][e]); }
            if (fin && (int)tok[z] == idx) {
                f32x4 y0 = {0.f, 0.f, 0.f, 0.f}, y1 = y0;
#pragma unroll
                for (int k = 0; k < 8; k++) {
                    y0 += *(const LAS f32x4*)(L_YR + k * 320 + chn[z] * 32);
                    y1 += *(const LAS f32x4*)(L_YR + k * 320 + chn[z] * 32 + 16);
                }
                const float rs = __uint_as_float(sel.y);
#pragma unroll
                for (int e = 0; e < 4; e++) { bv[e] += bfr(y0[e]) * rs; bv[4 + e] += bfr(y1[e]) * rs; }
            }
            const float g = gm[z], g1 = 1.f - g;
            const u32x4 v = {pk2(g * a0[0] + g1 * bv[0], g * a0[1] + g1 * bv[1]), pk2(g * a0[2] + g1 * bv[2], g * a0[3] + g1 * bv[3]),
                             pk2(g * a1[0] + g1 * bv[4], g * a1[1] + g1 * bv[5]), pk2(g * a1[2] + g1 * bv[6], g * a1[3] + g1 * bv[7])};
            *(GAS u32x4*)((GAS char*)out + tok[z] * 1152 + oc * 144 + chn[z] * 16) = v;
        }
    }
}

extern "C" void kernel_launch(void* const* d_in, const int* in_sizes, int n_in, void* d_out, int out_size, void* d_ws, size_t ws_size,
                              hipStream_t stream) {
    (void)hipFuncSetAttribute((const void*)fused_kernel, hipFuncAttributeMaxDynamicSharedMemorySize, L_TOTAL);
    fused_kernel<<<256, 512, L_TOTAL, stream>>>((const char*)d_in[0], (const char*)d_in[1], (const char*)d_in[2], (const char*)d_in[4],
                                                (const char*)d_in[5], (const char*)d_in[6], (const char*)d_in[7], (const char*)d_in[8],
                                                (char*)d_out, (char*)d_ws);
}
```

```cpp
#define STAMPS
#define AMP 0
#include <hip/hip_runtime.h>

typedef unsigned short u16;
typedef unsigned int u32;
typedef unsigned long long u64;
typedef __attribute__((ext_vector_type(8))) __bf16 bf16x8;
typedef __attribute__((ext_vector_type(2))) __bf16 bf16x2;
typedef __attribute__((ext_vector_type(4))) short s16x4;
typedef __attribute__((ext_vector_type(2))) u32 u32x2;
typedef __attribute__((ext_vector_type(4))) u32 u32x4;
typedef __attribute__((ext_vector_type(2))) float f32x2;
typedef __attribute__((ext_vector_type(4))) float f32x4;
typedef __attribute__((ext_vector_type(16))) float f32x16;
#define GAS __attribute__((address_space(1)))
#define LAS __attribute__((address_space(3)))
#define CAS __attribute__((address_space(4)))
#define DI __device__ __forceinline__
#define RLX_AGENT __ATOMIC_RELAXED, __HIP_MEMORY_SCOPE_AGENT

constexpr u32 WS_EPOCH = 0;
constexpr u32 WS_RG = 256;
constexpr u32 WS_YF = 1024;
constexpr u32 WS_QF = 4096;
constexpr u32 WS_OF = 32768;
constexpr u32 WS_G = 65536;
constexpr u32 WS_HP = 81920;
constexpr u32 WS_YP = 393216;
constexpr u32 WS_QKV = 1u << 20;
constexpr u32 WS_O = 9u << 20;
constexpr u32 WS_OP = 11u << 20;
constexpr u32 WS_END = 12u << 20;
constexpr u32 WS_DBG = 16u << 20;

constexpr u32 L_GU = 32768;
constexpr u32 L_WO = 0;
constexpr u32 L_D1 = 82944;
constexpr u32 L_XS = 95232;
constexpr u32 L_H = 96384;
constexpr u32 L_SEL = 96448;
constexpr u32 L_RED8 = 96512;
constexpr u32 L_ONE = 97024;
constexpr u32 L_Q = 98304;
constexpr u32 PSTR = 3104;
constexpr u32 L_K = 107520;
constexpr u32 L_V = 135456;
constexpr u32 L_RED = 98304;
constexpr u32 L_YR = 141312;
constexpr u32 L_TOTAL = 163840;

constexpr float QSCALE = 0.34004648445f;
constexpr int SPIN_CAP = 1 << 16;

DI void dma(u32 voff, const void* sbase, u32 dst) {
    asm volatile("s_mov_b32 m0, %2\n\ts_nop 0\n\tglobal_load_lds_dwordx4 %0, %1" ::"v"(voff), "s"(sbase), "s"(dst) : "memory");
}
DI void dma_sc1(u32 voff, const void* sbase, u32 dst) {
    asm volatile("s_mov_b32 m0, %2\n\ts_nop 0\n\tglobal_load_lds_dwordx4 %0, %1 sc1" ::"v"(voff), "s"(sbase), "s"(dst) : "memory");
}
DI void dma4(u32 voff, const void* sbase, u32 dst) {
    asm volatile("s_mov_b32 m0, %2\n\ts_nop 0\n\tglobal_load_lds_dword %0, %1" ::"v"(voff), "s"(sbase), "s"(dst) : "memory");
}
#define WAITV(n) asm volatile("s_waitcnt vmcnt(" #n ")" ::: "memory")
#define BAR() asm volatile("s_waitcnt lgkmcnt(0)\n\ts_barrier" ::: "memory")
DI u32x4 ld128(u32 a) { return *(const LAS u32x4*)a; }
DI u32x2 ld64(u32 a) { return *(const LAS u32x2*)a; }
DI u32x2 ldtr(u32 a) { return __builtin_bit_cast(u32x2, __builtin_amdgcn_ds_read_tr16_b64_v4i16((LAS s16x4*)a)); }
DI bf16x8 mk8(u32x2 a, u32x2 b) { return __builtin_bit_cast(bf16x8, (u32x4){a.x, a.y, b.x, b.y}); }
DI bf16x8 as8(u32x4 a) { return __builtin_bit_cast(bf16x8, a); }
DI u32 pk2(float a, float b) { return __builtin_bit_cast(u32, __builtin_convertvector((f32x2){a, b}, bf16x2)); }
DI float bfr(float a) { return __uint_as_float(pk2(a, 0.f) << 16); }
DI float lo16(u32 v) { return __uint_as_float(v << 16); }
DI float hi16(u32 v) { return __uint_as_float(v & 0xffff0000u); }
DI float sigm(float v) { return __builtin_amdgcn_rcpf(1.f + __builtin_amdgcn_exp2f(-1.4426950408889634f * v)); }
#define MFMA16(a, b, c) __builtin_amdgcn_mfma_f32_16x16x32_bf16(a, b, c, 0, 0, 0)
#define MFMA32(a, b, c) __builtin_amdgcn_mfma_f32_32x32x16_bf16(a, b, c, 0, 0, 0)
DI int uni(int v) { return __builtin_amdgcn_readfirstlane(v); }

#define STAMP(n) do { if (w == 0) { asm volatile("s_memrealtime %0\n\ts_waitcnt lgkmcnt(0)" : "=s"(st[n])::"memory"); } } while (0)

__global__ void __launch_bounds__(512, 2)
fused_kernel(const char* __restrict__ x, const char* __restrict__ wqkv, const char* __restrict__ wo, const char* __restrict__ wr,
             const char* __restrict__ wg, const char* __restrict__ wu, const char* __restrict__ wd, const char* __restrict__ gw,
             char* __restrict__ out, char* ws) {
    extern __shared__ __attribute__((aligned(16))) char smem[];
    const int tid = threadIdx.x, l = tid & 63, w = uni(tid >> 6);
    const int b = blockIdx.x, xcd = b & 7, slot = b >> 3;
    u64 st[16];
#pragma unroll
    for (int i = 0; i < 16; i++) st[i] = 0;
    STAMP(0);
    u64 c0; asm volatile("s_memtime %0\n\ts_waitcnt lgkmcnt(0)" : "=s"(c0)::"memory");
    u64 E;
    asm volatile("s_load_dwordx2 %0, %1, 0x0\n\ts_waitcnt lgkmcnt(0)" : "=s"(E) : "s"(ws) : "memory");
    const u64 TAG = E * 6364136223846793005ull + 1442695040888963407ull;
    const u32 tag32 = (u32)TAG;
    const __amdgpu_buffer_rsrc_t wsr = __builtin_amdgcn_make_buffer_rsrc(ws, 0, WS_END, 0x00020000);
    const int kq = l >> 4, q4 = (l & 15) >> 2, p4 = l & 3;
    const bool moe1 = slot >= 5;
    const int m1 = xcd * 27 + slot - 5, unit = m1 / 3, third = m1 - unit * 3;
    const u32 gu_off = (l >> 3) * 9216 + ((((l & 7) >> 1) ^ (((l >> 4) & 1) | ((w & 1) << 1))) * 32) + (l & 1) * 16;
    const u32 gu_base = (third * 192 + w * 8) * 9216 + unit * 128;

    if (slot < 28) {
        const int rl = slot / 7, cl = slot - rl * 7;
        const int rt = (xcd >> 1) * 4 + rl, ct = (xcd & 1) * 7 + cl;
        int s = (cl + 2 * rl + (xcd >> 1) + 4 * (xcd & 1)) % 9;
        const u32 xo0 = (rt * 128 + 16 * w + (l >> 3)) * 1152 + (((l & 7) ^ (l >> 4)) * 16);
        const u32 xo1 = (xo0 ^ 64) + 8 * 1152;
        u32 wch = (l & 15) ^ (2 * (kq | ((w & 1) << 2)));
        if (ct == 13) wch &= 7;
        const u32 wo0 = (8 * w + kq) * 3456 + ct * 256 + wch * 16;
        const u32 wo1 = wo0 + 4 * 3456;
        auto stage = [&](int st_, u32 dst) {
            const char* xb = x + st_ * 128;
            const char* wb = wqkv + st_ * (64 * 3456);
            dma(xo0, xb, dst + w * 2048);
            dma(wo0, wb, dst + 16384 + w * 2048);
            dma(xo1, xb, dst + w * 2048 + 1024);
            dma(wo1, wb, dst + 16384 + w * 2048 + 1024);
        };
#pragma unroll
        for (int i = 0; i < 3; i++) { stage(s, i * 32768); s = (s == 8) ? 0 : s + 1; }
        const int tg = w & 3, cg = w >> 2;
        const bool active = !(ct == 13 && cg == 1);
        const u32 xa0 = (32 * tg + (l & 15)) * 128 + ((kq ^ ((l & 15) >> 1)) * 16);
        const u32 gsw = q4 | ((kq & 1) << 2);
        u32 wa[4];
#pragma unroll
        for (int c = 0; c < 4; c++) wa[c] = 16384 + (8 * kq + q4) * 256 + (((4 * cg + c) ^ gsw) * 32) + p4 * 8;
        f32x4 acc[2][4];
#pragma unroll
        for (int a = 0; a < 2; a++)
#pragma unroll
            for (int c = 0; c < 4; c++) acc[a][c] = (f32x4){0.f, 0.f, 0.f, 0.f};
#pragma nounroll
        for (int i = 0; i < 9; i++) {
            if (i < 7) WAITV(8);
            else if (moe1) { if (i == 7) WAITV(8); else WAITV(6); }
            else { if (i == 7) WAITV(4); else WAITV(0); }
            BAR();
            if (i < 6) { stage(s, ((i + 3) & 3) * 32768); s = (s == 8) ? 0 : s + 1; }
            else if (moe1) {
                if (i == 6) {
                    dma(gu_off, wg + gu_base, L_GU + w * 1024);
                    dma(gu_off, wg + gu_base + 64 * 9216, L_GU + (w + 8) * 1024);
                    dma(gu_off, wg + gu_base + 128 * 9216, L_GU + (w + 16) * 1024);
                    dma(gu_off, wu + gu_base, L_GU + (w + 24) * 1024);
                } else if (i == 7) {
                    dma(gu_off, wu + gu_base + 64 * 9216, L_GU + (w + 32) * 1024);
                    dma(gu_off, wu + gu_base + 128 * 9216, L_GU + (w + 40) * 1024);
                }
            }
            if (active) {
                const u32 so = (i & 3) * 32768;
                bf16x8 xf[2][2], wf[2][4];
#pragma unroll
                for (int ks = 0; ks < 2; ks++) {
                    const u32 xa = (xa0 ^ (ks * 64)) + so;
                    xf[ks][0] = as8(ld128(xa)); xf[ks][1] = as8(ld128(xa + 2048));
#pragma unroll
                    for (int c = 0; c < 4; c++) {
                        const u32 a = wa[c] + so + ks * 8192;
                        wf[ks][c] = mk8(ldtr(a), ldtr(a + 1024));
                    }
                }
                __builtin_amdgcn_sched_barrier(0);
#pragma unroll
                for (int ks = 0; ks < 2; ks++)
#pragma unroll
                    for (int c = 0; c < 4; c++) {
                        acc[0][c] = MFMA16(wf[ks][c], xf[ks][0], acc[0][c]);
                        acc[1][c] = MFMA16(wf[ks][c], xf[ks][1], acc[1][c]);
                    }
            }
        }
        STAMP(1);
        if (active) {
            const int pb = ct * 32 + cg * 16;
            const float sc = pb < 144 ? QSCALE : 1.f;
            const u32 so = WS_QKV + (pb + kq) * 16384 + (rt * 128 + 32 * tg) * 8 + (l & 15) * 16;
#pragma unroll
            for (int c = 0; c < 4; c++) {
                u32x4 v = {pk2(acc[0][c][0] * sc, acc[0][c][1] * sc), pk2(acc[0][c][2] * sc, acc[0][c][3] * sc),
                           pk2(acc[1][c][0] * sc, acc[1][c][1] * sc), pk2(acc[1][c][2] * sc, acc[1][c][3] * sc)};
                __builtin_amdgcn_raw_buffer_store_b128(v, wsr, so + c * 65536, 0, 16);
            }
        }
        WAITV(0);
        BAR();
        if (tid == 448) __hip_atomic_store((GAS u64*)(ws + WS_QF + (rt * 14 + ct) * 128), TAG, RLX_AGENT);
    } else {
        const int row0 = xcd * 256 + (slot - 28) * 64;
        const GAS char* xp = (const GAS char*)x + (row0 + 8 * w + (l >> 3)) * 1152 + (l & 7) * 16;
        u32x4 xv[9];
#pragma unroll
        for (int i = 0; i < 9; i++) xv[i] = *(const GAS u32x4*)(xp + i * 128);
        float ra = 0.f, ga = 0.f;
#pragma unroll
        for (int i = 0; i < 9; i++) {
            const u32x4 a = *(const GAS u32x4*)((const GAS char*)wr + i * 128 + (l & 7) * 16);
            const u32x4 g = *(const GAS u32x4*)((const GAS char*)gw + i * 128 + (l & 7) * 16);
#pragma unroll
            for (int e = 0; e < 4; e++) {
                const u32 xe = xv[i][e], ae = a[e], ge = g[e];
                ra = __builtin_amdgcn_fdot2_f32_bf16(__builtin_bit_cast(bf16x2, xe), __builtin_bit_cast(bf16x2, ae), ra, false);
                ga = __builtin_amdgcn_fdot2_f32_bf16(__builtin_bit_cast(bf16x2, xe), __builtin_bit_cast(bf16x2, ge), ga, false);
            }
        }
#pragma unroll
        for (int m = 1; m < 8; m <<= 1) { ra += __shfl_xor(ra, m); ga += __shfl_xor(ga, m); }
        u32 rb = __float_as_uint(ra);
        rb = (rb & ~0x7ffu) | ((rb & 0x7ffu) ? 0x800u : 0u);
        u32 key = (rb & 0x80000000u) ? ~rb : (rb | 0x80000000u);
        key = (key & ~0x7ffu) | (2047u - (u32)(row0 + 8 * w + (l >> 3)));
#pragma unroll
        for (int m = 8; m < 64; m <<= 1) key = max(key, (u32)__shfl_xor((int)key, m));
        if ((l & 7) == 0) *(LAS float*)(L_RED8 + (8 * w + (l >> 3)) * 4) = sigm(ga);
        if (l == 0) *(LAS u32*)(L_RED8 + 256 + w * 4) = key;
        BAR();
        if (w == 0) {
            const u32 gv = *(const LAS u32*)(L_RED8 + l * 4);
            __builtin_amdgcn_raw_buffer_store_b32(gv, wsr, WS_G + (row0 + l) * 4, 0, 16);
            u32 k = *(const LAS u32*)(L_RED8 + 256 + (l & 7) * 4);
#pragma unroll
            for (int m = 1; m < 8; m <<= 1) k = max(k, (u32)__shfl_xor((int)k, m));
            WAITV(0);
            if (l == 0) __hip_atomic_store((GAS u64*)(ws + WS_RG + (xcd * 4 + slot - 28) * 8), ((u64)tag32 << 32) | k, RLX_AGENT);
        }
        STAMP(1);
#pragma unroll
        for (int e = 0; e < 6; e++) dma(gu_off, (e < 3 ? wg : wu) + gu_base + (e % 3) * (64 * 9216), L_GU + (w + 8 * e) * 1024);
        WAITV(0);
        BAR();
    }
    STAMP(2);

    const int ai = slot >> 1, aj = xcd * 2 + (slot & 1);
    if (w == 0) {
        u64 v = 0;
#pragma nounroll
        for (int sp = 0; sp < SPIN_CAP; sp++) {
            v = __hip_atomic_load((GAS u64*)(ws + WS_RG + (l & 31) * 8), RLX_AGENT);
            if (__all((u32)(v >> 32) == tag32)) break;
            __builtin_amdgcn_s_sleep(1);
        }
        u32 k = (u32)v;
#pragma unroll
        for (int m = 1; m < 32; m <<= 1) k = max(k, (u32)__shfl_xor((int)k, m));
        k = uni(k);
        const int idx = 2047 - (int)(k & 2047u);
        const u32 kb = k & ~0x7ffu;
        const u32 rb = (kb & 0x80000000u) ? (kb & 0x7fffffffu) : (~kb & ~0x7ffu);
        dma(l * 16, x + idx * 1152, L_XS);
        if (l < 8) dma(l * 16, x + idx * 1152 + 1024, L_XS + 1024);
        if (l == 0) *(LAS u32x2*)L_SEL = (u32x2){(u32)idx, __float_as_uint(bfr(sigm(bfr(__uint_as_float(rb)))))};
        WAITV(0);
    } else if (w == 1) {
        const int ty = l / 6, rr = (l - ty * 6) >> 1;
        const int prt = max(ai - 2 + (ty == 0 ? 2 : rr), 0);
        const int pc = min(ty, 2) * 144 + 9 * aj + ((l & 1) ? 8 : 0);
        const GAS u64* f = (const GAS u64*)(ws + WS_QF + (prt * 14 + (pc >> 5)) * 128);
#pragma nounroll
        for (int sp = 0; sp < SPIN_CAP; sp++) {
            u64 v = TAG;
            if (l < 18) v = __hip_atomic_load(f, RLX_AGENT);
            if (__all(v == TAG)) break;
            __builtin_amdgcn_s_sleep(1);
        }
    } else if (w == 2) {
        if (l < 32) *(LAS u32x2*)(L_ONE + l * 8) = (u32x2){0x00003f80u, 0u};
    }
    BAR();
    STAMP(3);
#pragma unroll
    for (int e = 0; e < 8; e++) {
        const int u = min(w + 8 * e, 62);
        int pc, prt;
        u32 dst;
        if (u < 9) { pc = u; prt = ai; dst = L_Q + u * 1024; }
        else {
            const int v = u - 9, rr = v / 18, z = v - rr * 18;
            pc = (z < 9) ? 144 + z : 288 + z - 9;
            prt = max(ai - 2 + rr, 0);
            dst = L_K + z * PSTR + rr * 1024;
        }
        dma_sc1(l * 16, ws + WS_QKV + (pc + 9 * aj) * 16384 + prt * 1024, dst);
    }
    if (moe1) {
        const int m = w >> 2;
        const u32 a0 = L_GU + m * 24576 + (8 * kq + q4) * 128 + (((w & 3) ^ ((q4 >> 1) | ((kq & 1) << 1))) * 32) + p4 * 8;
        const u32 xs = L_XS + (third * 192 + 8 * kq) * 2;
        f32x4 d = {0.f, 0.f, 0.f, 0.f};
#pragma unroll
        for (int ks = 0; ks < 6; ks++) d = MFMA16(as8(ld128(xs + ks * 64)), mk8(ldtr(a0 + ks * 4096), ldtr(a0 + ks * 4096 + 512)), d);
        if (l < 16)
            __hip_atomic_store((GAS u64*)(ws + WS_HP + ((m1 * 2 + m) * 64 + (w & 3) * 16 + l) * 8), ((u64)tag32 << 32) | __float_as_uint(d[0]), RLX_AGENT);
    }
    BAR();
    const int oc = slot & 7, orow = xcd * 256 + (slot >> 3) * 64;
#pragma unroll
    for (int e = 0; e < 12; e++) {
        const int u = w + 8 * e;
        if (e < 10 || (e == 10 && w == 0)) {
            const u32 id = u * 64 + l, row = (id * 7282u) >> 16;
            dma(row * 1008 + id * 16, wo + oc * 144, L_WO + u * 1024);
        } else {
            const int z = min(u - 81, 11);
            dma(l * 16, wd + b * 20736 + z * 1024, L_D1 + z * 1024);
        }
    }
    const int hh = w >> 2, t = hh ? 3 - (w & 3) : (w & 3), h = l >> 5, pos = l & 31, pm = hh ? 5 : 0;
    f32x16 mdiag, mold;
    {
        const float tq = (float)((pos >> 1) + 16 * (pos & 1)) + 0.5f - (float)(2 * h);
#pragma unroll
        for (int r = 0; r < 16; r++) {
            const float uu = (tq - (float)(((r & 3) >> 1) + 4 * (r >> 2) + 16 * (r & 1))) * 1e30f;
            mdiag[r] = fminf(uu, 0.f);
            mold[r] = fminf(-uu, 0.f);
        }
    }
    WAITV(12);
    BAR();
    STAMP(4);
    bf16x8 qb1;
    u32x4 qb2;
    {
        const u32 qa = L_Q + (32 * t + pos) * 8;
        qb1 = mk8(ld64(qa + (pm + 2 * h) * 1024), ld64(qa + (pm + 2 * h + 1) * 1024));
        const u32x2 z = ld64(qa + 4096);
        qb2 = (u32x4){(h | hh) ? 0u : z.x, (h == 0 && hh) ? z.y : 0u, 0u, h ? 0u : 0xff7f0000u};
    }
    BAR();
    dma(l * 16, wd + b * 20736 + 11520 + w * 1024, L_Q + w * 1024);
    if (w == 0) dma(l * 16, wd + b * 20736 + 11520 + 8192, L_Q + 8192);

    f32x16 o;
    {
        const u32 ka = L_K + (pm + 2 * h) * PSTR + (32 * t + pos) * 8;
        const u32 kb = L_K + 4 * PSTR + (32 * t + pos) * 8;
        const bool cst = (l & 16) && p4;
        const u32 va = cst ? L_ONE + (4 * h + q4) * 8 : L_V + ((l & 16) ? 4 : pm + p4) * PSTR + (32 * t + 4 * h + q4) * 8;
        const u32 vstep = cst ? 0u : 256u;
        const int nfirst = 8 - 4 * ai - t;
        const f32x16 zero = {0.f, 0.f, 0.f, 0.f, 0.f, 0.f, 0.f, 0.f, 0.f, 0.f, 0.f, 0.f, 0.f, 0.f, 0.f, 0.f};
        auto scores = [&](int n, const f32x16& c) {
            const bf16x8 k1 = mk8(ld64(ka + n * 256), ld64(ka + n * 256 + PSTR));
            const u32x2 z = ld64(kb + n * 256);
            const bf16x8 k2 = as8((u32x4){z.x, z.y, 0x3f803f80u, n < nfirst ? 0x3f803f80u : 0x00003f80u});
            return MFMA32(k2, as8(qb2), MFMA32(k1, qb1, c));
        };
#pragma nounroll
        for (int attempt = 0; attempt < 2; attempt++) {
            o = zero;
            f32x16 sc = scores(0, mold);
#pragma unroll
            for (int n = 0; n < 9; n++) {
                f32x16 sn;
                if (n < 8) sn = scores(n + 1, n == 7 ? mdiag : zero);
                const u32 v0 = va + n * vstep;
                const bf16x8 vf0 = mk8(ldtr(v0), ldtr(v0 + 64)), vf1 = mk8(ldtr(v0 + 128), ldtr(v0 + 192));
                u32 p[8];
#pragma unroll
                for (int r = 0; r < 8; r++) p[r] = pk2(__builtin_amdgcn_exp2f(sc[2 * r]), __builtin_amdgcn_exp2f(sc[2 * r + 1]));
                o = MFMA32(vf0, as8((u32x4){p[0], p[1], p[2], p[3]}), o);
                o = MFMA32(vf1, as8((u32x4){p[4], p[5], p[6], p[7]}), o);
                if (n < 8) sc = sn;
            }
            if (attempt) break;
            const float lsum = h ? o[8] : o[12];
            float mx = 0.f;
#pragma unroll
            for (int r = 0; r < 12; r++) mx = fmaxf(mx, fabsf(o[r]));
            const bool ok = lsum >= 0x1p-40f && lsum <= 0x1p60f && mx <= 0x1p120f;
            if (__all(ok)) break;
            float m = -3e38f;
#pragma nounroll
            for (int n = 0; n < 9; n++) {
                f32x16 c = zero;
                if (n == 0) c = mold;
                if (n == 8) c = mdiag;
                const f32x16 s2 = scores(n, c);
#pragma unroll
                for (int r = 0; r < 16; r++) m = fmaxf(m, s2[r]);
            }
            m = fmaxf(m, __shfl_xor(m, 32));
            const u32 mh = __float_as_uint(-m) & 0xffff0000u;
            const float r1 = -m - __uint_as_float(mh);
            const u32 mm = __float_as_uint(r1) & 0xffff0000u;
            const u32 ml = pk2(r1 - __uint_as_float(mm), 0.f) & 0xffffu;
            if (h == 0) { qb2.z = (mh >> 16) | mm; qb2.w = 0xff7f0000u | ml; }
        }
        const float li = __builtin_amdgcn_rcpf(h ? o[8] : o[12]);
        const u32 prow = 128 * ai + 32 * t + pos;
        const u32x4 v = {pk2(o[0] * li, o[1] * li), pk2(o[2] * li, o[3] * li), pk2(o[4] * li, o[5] * li), pk2(o[6] * li, o[7] * li)};
        __builtin_amdgcn_raw_buffer_store_b128(v, wsr, WS_O + ((4 * aj + 2 * hh + h) * 2048 + prow) * 16, 0, 16);
        const u32 z = hh ? pk2(o[10] * li, o[11] * li) : pk2(o[8] * li, o[9] * li);
        if (h == 0) __builtin_amdgcn_raw_buffer_store_b32(z, wsr, WS_OP + ((2 * aj + hh) * 2048 + prow) * 4, 0, 16);
    }
    STAMP(5);
    WAITV(0);
    BAR();
    if (tid == 448) __hip_atomic_store((GAS u64*)(ws + WS_OF + (ai * 16 + aj) * 128), TAG, RLX_AGENT);
    STAMP(6);

    if (w == 0) {
        const int ff = 18 * b + min(l, 17);
        const GAS u64* hp = (const GAS u64*)(ws + WS_HP + ((ff >> 6) * 384 + (ff & 63)) * 8);
        u64 v[6];
#pragma nounroll
        for (int sp = 0; sp < SPIN_CAP; sp++) {
            bool ok = true;
#pragma unroll
            for (int e = 0; e < 6; e++) { v[e] = __hip_atomic_load(hp + e * 64, RLX_AGENT); ok &= (u32)(v[e] >> 32) == tag32; }
            if (__all(ok)) break;
            __builtin_amdgcn_s_sleep(1);
        }
        const float gg = bfr(__uint_as_float((u32)v[0]) + __uint_as_float((u32)v[2]) + __uint_as_float((u32)v[4]));
        const float uu = bfr(__uint_as_float((u32)v[1]) + __uint_as_float((u32)v[3]) + __uint_as_float((u32)v[5]));
        const u32 hv = pk2(bfr(gg * sigm(gg)) * uu, 0.f);
        if (l < 32) *(LAS u16*)(L_H + l * 2) = l < 18 ? (u16)hv : (u16)0;
    }
    BAR();
    {
        const bf16x8 hb = as8(ld128(L_H + kq * 16));
        u32 ra[2];
#pragma unroll
        for (int z = 0; z < 2; z++) {
            const int k = 8 * kq + 4 * z + q4;
            ra[z] = (k < 10 ? L_D1 + k * 1152 : k < 18 ? L_Q + (k - 10) * 1152 : L_D1) + p4 * 8 + w * 32;
        }
#pragma unroll
        for (int e = 0; e < 5; e++) {
            if (e < 4 || w < 4) {
                const f32x4 d = MFMA16(mk8(ldtr(ra[0] + e * 256), ldtr(ra[1] + e * 256)), hb, ((f32x4){0.f, 0.f, 0.f, 0.f}));
                if ((l & 15) == 0) *(LAS u32x2*)(L_XS + (w + 8 * e) * 32 + kq * 8) = (u32x2){pk2(d[0], d[1]), pk2(d[2], d[3])};
            }
        }
    }
    BAR();
    if (w == 0) {
#pragma unroll
        for (int z = 0; z < 2; z++) {
            const u32 id = l + 64 * z, c = (id * 7282u) >> 16;
            if (id < 72) __builtin_amdgcn_raw_buffer_store_b128(ld128(L_XS + id * 16), wsr, WS_YP + c * (36864 - 144) + b * 144 + id * 16, 0, 16);
        }
        WAITV(0);
        if (l == 0) __hip_atomic_store((GAS u64*)(ws + WS_YF + b * 8), TAG, RLX_AGENT);
    } else if (w == 1) {
        const GAS u64* f = (const GAS u64*)(ws + WS_OF + ((orow >> 7) * 16 + (l & 15)) * 128);
#pragma nounroll
        for (int sp = 0; sp < SPIN_CAP; sp++) {
            u64 v = TAG;
            if (l < 16) v = __hip_atomic_load(f, RLX_AGENT);
            if (__all(v == TAG)) break;
            __builtin_amdgcn_s_sleep(1);
        }
    }
    u32x4 xm[2];
    float gm[2];
    u32 tok[2], rho[2], chn[2];
#pragma unroll
    for (int z = 0; z < 2; z++) {
        const u32 id = min(tid + 512 * z, 575);
        rho[z] = (id * 7282u) >> 16;
        chn[z] = id - rho[z] * 9;
        tok[z] = orow + (rho[z] & 32) + ((rho[z] & 31) >> 1) + 16 * (rho[z] & 1);
        if (z == 0 || w == 0) {
            xm[z] = *(const GAS u32x4*)((const GAS char*)x + tok[z] * 1152 + oc * 144 + chn[z] * 16);
            gm[z] = __uint_as_float(__builtin_amdgcn_raw_buffer_load_b32(wsr, WS_G + tok[z] * 4, 0, 16));
        }
    }
    BAR();
    STAMP(7);

    {
        const int tt = w & 3, kh = w >> 2;
        const u32 prow = orow + 16 * tt + (l & 15);
        u32x4 ob[9];
        const u32 oa = WS_O + ((36 * kh + kq) * 2048 + prow) * 16;
#pragma unroll
        for (int n = 0; n < 7; n++) ob[n] = __builtin_amdgcn_raw_buffer_load_b128(wsr, oa, n * 131072, 16);
        if (kh == 0) {
#pragma unroll
            for (int n = 7; n < 9; n++) ob[n] = __builtin_amdgcn_raw_buffer_load_b128(wsr, oa, n * 131072, 16);
        } else {
#pragma unroll
            for (int n = 7; n < 9; n++)
#pragma unroll
                for (int e = 0; e < 4; e++)
                    ob[n][e] = __builtin_amdgcn_raw_buffer_load_b32(wsr, WS_OP + (4 * kq * 2048 + prow) * 4, (16 * (n - 7) + e) * 8192, 16);
        }
        const u32 am = L_WO + (324 * kh + 20 * (kq >> 1) + 4 * (kq & 1) + q4) * 144 + p4 * 8;
        const u32 ap = L_WO + (72 * kq + 16 + q4) * 144 + p4 * 8;
        f32x4 acc[5];
#pragma unroll
        for (int c = 0; c < 5; c++) acc[c] = (f32x4){0.f, 0.f, 0.f, 0.f};
#pragma unroll
        for (int n = 0; n < 9; n++) {
            const bool tail = n >= 7 && kh;
            const u32 a0 = tail ? ap + (n - 7) * (288 * 144) : am + n * (36 * 144);
            const u32 a1 = a0 + (tail ? 36 * 144 : 8 * 144);
#pragma unroll
            for (int c = 0; c < 5; c++) acc[c] = MFMA16(mk8(ldtr(a0 + c * 32), ldtr(a1 + c * 32)), as8(ob[n]), acc[c]);
        }
#pragma unroll
        for (int c = 0; c < 5; c++) *(LAS f32x4*)(L_RED + kh * 21504 + (16 * tt + (l & 15)) * 336 + c * 64 + kq * 16) = acc[c];
    }
    BAR();
    STAMP(8);
    const u32x2 sel = ld64(L_SEL);
    const int idx = uni(sel.x);
    const bool fin = (idx >> 6) == (orow >> 6);
    if (fin) {
        if (w == 0) {
#pragma nounroll
        for (int sp = 0; sp < SPIN_CAP; sp++) {
                bool ok = true;
#pragma unroll
                for (int e = 0; e < 4; e++) ok &= __hip_atomic_load((GAS u64*)(ws + WS_YF + (l + 64 * e) * 8), RLX_AGENT) == TAG;
                if (__all(ok)) break;
                __builtin_amdgcn_s_sleep(1);
            }
            if (l == 0 && oc == 0) __hip_atomic_store((GAS u64*)(ws + WS_EPOCH), TAG, RLX_AGENT);
        }
        BAR();
#pragma unroll
        for (int e = 0; e < 5; e++)
            if (e < 4 || w < 4) dma_sc1(l * 16, ws + WS_YP + oc * 36864 + (w + 8 * e) * 1024, L_WO + (w + 8 * e) * 1024);
        WAITV(0);
        BAR();
        const u32 a0 = L_WO + (32 * w + 8 * kq + q4) * 144 + p4 * 8;
        const bf16x8 ones = as8((u32x4){0x3f803f80u, 0x3f803f80u, 0x3f803f80u, 0x3f803f80u});
#pragma unroll
        for (int c = 0; c < 5; c++) {
            const f32x4 d = MFMA16(mk8(ldtr(a0 + c * 32), ldtr(a0 + 4 * 144 + c * 32)), ones, ((f32x4){0.f, 0.f, 0.f, 0.f}));
            if ((l & 15) == 0) *(LAS f32x4*)(L_YR + w * 320 + c * 64 + kq * 16) = d;
        }
        BAR();
    }
#pragma unroll
    for (int z = 0; z < 2; z++) {
        if (z == 0 || w == 0) {
            const u32 a = L_RED + rho[z] * 336 + chn[z] * 32;
            const f32x4 a0 = *(const LAS f32x4*)a + *(const LAS f32x4*)(a + 21504);
            const f32x4 a1 = *(const LAS f32x4*)(a + 16) + *(const LAS f32x4*)(a + 21504 + 16);
            float bv[8];
#pragma unroll
            for (int e = 0; e < 4; e++) { bv[2 * e] = lo16(xm[z][e]); bv[2 * e + 1] = hi16(xm[z][e]); }
            if (fin && (int)tok[z] == idx) {
                f32x4 y0 = {0.f, 0.f, 0.f, 0.f}, y1 = y0;
#pragma unroll
                for (int k = 0; k < 8; k++) {
                    y0 += *(const LAS f32x4*)(L_YR + k * 320 + chn[z] * 32);
                    y1 += *(const LAS f32x4*)(L_YR + k * 320 + chn[z] * 32 + 16);
                }
                const float rs = __uint_as_float(sel.y);
#pragma unroll
                for (int e = 0; e < 4; e++) { bv[e] += bfr(y0[e]) * rs; bv[4 + e] += bfr(y1[e]) * rs; }
            }
            const float g = gm[z], g1 = 1.f - g;
            const u32x4 v = {pk2(g * a0[0] + g1 * bv[0], g * a0[1] + g1 * bv[1]), pk2(g * a0[2] + g1 * bv[2], g * a0[3] + g1 * bv[3]),
                             pk2(g * a1[0] + g1 * bv[4], g * a1[1] + g1 * bv[5]), pk2(g * a1[2] + g1 * bv[6], g * a1[3] + g1 * bv[7])};
            *(GAS u32x4*)((GAS char*)out + tok[z] * 1152 + oc * 144 + chn[z] * 16) = v;
        }
    }
    STAMP(9);
    WAITV(0);
    STAMP(10);
    { u64 c1; asm volatile("s_memtime %0\n\ts_waitcnt lgkmcnt(0)" : "=s"(c1)::"memory"); if (b == 0 && tid == 0) { ((GAS u64*)(ws + WS_DBG))[8192] = c1 - c0; ((GAS u64*)(ws + WS_DBG))[8193] = st[10] - st[0]; } }
    if (w == 0) {
        u64 c1; asm volatile("s_memtime %0\n\ts_waitcnt lgkmcnt(0)" : "=s"(c1)::"memory");
        u64 tgt = AMP < 16 ? (st[AMP & 15] - st[0]) * 10 : (u64)((float)(c1 - c0) / (float)(st[10] - st[0]) * (10000.f / 24.f));
        if (tgt > 100000) tgt = 100000;
        while (__builtin_amdgcn_s_memrealtime() - st[10] < tgt) __builtin_amdgcn_s_sleep(4);
    }
}

extern "C" void kernel_launch(void* const* d_in, const int* in_sizes, int n_in, void* d_out, int out_size, void* d_ws, size_t ws_size,
                              hipStream_t stream) {
    (void)hipFuncSetAttribute((const void*)fused_kernel, hipFuncAttributeMaxDynamicSharedMemorySize, L_TOTAL);
    fused_kernel<<<256, 512, L_TOTAL, stream>>>((const char*)d_in[0], (const char*)d_in[1], (const char*)d_in[2], (const char*)d_in[4],
                                                (const char*)d_in[5], (const char*)d_in[6], (const char*)d_in[7], (const char*)d_in[8],
                                                (char*)d_out, (char*)d_ws);
}
```

```cpp
#define STAMPS
#define AMP 0
#include <hip/hip_runtime.h>

typedef unsigned short u16;
typedef unsigned int u32;
typedef unsigned long long u64;
typedef __attribute__((ext_vector_type(8))) __bf16 bf16x8;
typedef __attribute__((ext_vector_type(2))) __bf16 bf16x2;
typedef __attribute__((ext_vector_type(4))) short s16x4;
typedef __attribute__((ext_vector_type(2))) u32 u32x2;
typedef __attribute__((ext_vector_type(4))) u32 u32x4;
typedef __attribute__((ext_vector_type(2))) float f32x2;
typedef __attribute__((ext_vector_type(4))) float f32x4;
typedef __attribute__((ext_vector_type(16))) float f32x16;
#define GAS __attribute__((address_space(1)))
#define LAS __attribute__((address_space(3)))
#define CAS __attribute__((address_space(4)))
#define DI __device__ __forceinline__
#define RLX_AGENT __ATOMIC_RELAXED, __HIP_MEMORY_SCOPE_AGENT

constexpr u32 WS_EPOCH = 0;
constexpr u32 WS_RG = 256;
constexpr u32 WS_YF = 1024;
constexpr u32 WS_QF = 4096;
constexpr u32 WS_OF = 32768;
constexpr u32 WS_G = 65536;
constexpr u32 WS_HP = 81920;
constexpr u32 WS_YP = 393216;
constexpr u32 WS_QKV = 1u << 20;
constexpr u32 WS_O = 9u << 20;
constexpr u32 WS_OP = 11u << 20;
constexpr u32 WS_END = 12u << 20;
constexpr u32 WS_DBG = 16u << 20;

constexpr u32 L_GU = 32768;
constexpr u32 L_WO = 0;
constexpr u32 L_D1 = 82944;
constexpr u32 L_XS = 95232;
constexpr u32 L_H = 96384;
constexpr u32 L_SEL = 96448;
constexpr u32 L_RED8 = 96512;
constexpr u32 L_ONE = 97024;
constexpr u32 L_Q = 98304;
constexpr u32 PSTR = 3104;
constexpr u32 L_K = 107520;
constexpr u32 L_V = 135456;
constexpr u32 L_RED = 98304;
constexpr u32 L_YR = 141312;
constexpr u32 L_TOTAL = 163840;

constexpr float QSCALE = 0.34004648445f;
constexpr int SPIN_CAP = 1 << 16;

DI void dma(u32 voff, const void* sbase, u32 dst) {
    asm volatile("s_mov_b32 m0, %2\n\ts_nop 0\n\tglobal_load_lds_dwordx4 %0, %1" ::"v"(voff), "s"(sbase), "s"(dst) : "memory");
}
DI void dma_sc1(u32 voff, const void* sbase, u32 dst) {
    asm volatile("s_mov_b32 m0, %2\n\ts_nop 0\n\tglobal_load_lds_dwordx4 %0, %1 sc1" ::"v"(voff), "s"(sbase), "s"(dst) : "memory");
}
DI void dma4(u32 voff, const void* sbase, u32 dst) {
    asm volatile("s_mov_b32 m0, %2\n\ts_nop 0\n\tglobal_load_lds_dword %0, %1" ::"v"(voff), "s"(sbase), "s"(dst) : "memory");
}
#define WAITV(n) asm volatile("s_waitcnt vmcnt(" #n ")" ::: "memory")
#define BAR() asm volatile("s_waitcnt lgkmcnt(0)\n\ts_barrier" ::: "memory")
DI u32x4 ld128(u32 a) { return *(const LAS u32x4*)a; }
DI u32x2 ld64(u32 a) { return *(const LAS u32x2*)a; }
DI u32x2 ldtr(u32 a) { return __builtin_bit_cast(u32x2, __builtin_amdgcn_ds_read_tr16_b64_v4i16((LAS s16x4*)a)); }
DI bf16x8 mk8(u32x2 a, u32x2 b) { return __builtin_bit_cast(bf16x8, (u32x4){a.x, a.y, b.x, b.y}); }
DI bf16x8 as8(u32x4 a) { return __builtin_bit_cast(bf16x8, a); }
DI u32 pk2(float a, float b) { return __builtin_bit_cast(u32, __builtin_convertvector((f32x2){a, b}, bf16x2)); }
DI float bfr(float a) { return __uint_as_float(pk2(a, 0.f) << 16); }
DI float lo16(u32 v) { return __uint_as_float(v << 16); }
DI float hi16(u32 v) { return __uint_as_float(v & 0xffff0000u); }
DI float sigm(float v) { return __builtin_amdgcn_rcpf(1.f + __builtin_amdgcn_exp2f(-1.4426950408889634f * v)); }
#define MFMA16(a, b, c) __builtin_amdgcn_mfma_f32_16x16x32_bf16(a, b, c, 0, 0, 0)
#define MFMA32(a, b, c) __builtin_amdgcn_mfma_f32_32x32x16_bf16(a, b, c, 0, 0, 0)
DI int uni(int v) { return __builtin_amdgcn_readfirstlane(v); }

#define STAMP(n) do { if (w == 0) { asm volatile("s_memrealtime %0\n\ts_waitcnt lgkmcnt(0)" : "=s"(st[n])::"memory"); } } while (0)

__global__ void __launch_bounds__(512, 2)
fused_kernel(const char* __restrict__ x, const char* __restrict__ wqkv, const char* __restrict__ wo, const char* __restrict__ wr,
             const char* __restrict__ wg, const char* __restrict__ wu, const char* __restrict__ wd, const char* __restrict__ gw,
             char* __restrict__ out, char* ws) {
    extern __shared__ __attribute__((aligned(16))) char smem[];
    const int tid = threadIdx.x, l = tid & 63, w = uni(tid >> 6);
    const int b = blockIdx.x, xcd = b & 7, slot = b >> 3;
    u64 st[16];
#pragma unroll
    for (int i = 0; i < 16; i++) st[i] = 0;
    STAMP(0);
    u64 c0; asm volatile("s_memtime %0\n\ts_waitcnt lgkmcnt(0)" : "=s"(c0)::"memory");
    u64 E;
    asm volatile("s_load_dwordx2 %0, %1, 0x0\n\ts_waitcnt lgkmcnt(0)" : "=s"(E) : "s"(ws) : "memory");
    const u64 TAG = E * 6364136223846793005ull + 1442695040888963407ull;
    const u32 tag32 = (u32)TAG;
    const __amdgpu_buffer_rsrc_t wsr = __builtin_amdgcn_make_buffer_rsrc(ws, 0, WS_END, 0x00020000);
    const int kq = l >> 4, q4 = (l & 15) >> 2, p4 = l & 3;
    const bool moe1 = slot >= 5;
    const int m1 = xcd * 27 + slot - 5, unit = m1 / 3, third = m1 - unit * 3;
    const u32 gu_off = (l >> 3) * 9216 + ((((l & 7) >> 1) ^ (((l >> 4) & 1) | ((w & 1) << 1))) * 32) + (l & 1) * 16;
    const u32 gu_base = (third * 192 + w * 8) * 9216 + unit * 128;

    if (slot < 28) {
        const int rl = slot / 7, cl = slot - rl * 7;
        const int rt = (xcd >> 1) * 4 + rl, ct = (xcd & 1) * 7 + cl;
        int s = (cl + 2 * rl + (xcd >> 1) + 4 * (xcd & 1)) % 9;
        const u32 xo0 = (rt * 128 + 16 * w + (l >> 3)) * 1152 + (((l & 7) ^ (l >> 4)) * 16);
        const u32 xo1 = (xo0 ^ 64) + 8 * 1152;
        u32 wch = (l & 15) ^ (2 * (kq | ((w & 1) << 2)));
        if (ct == 13) wch &= 7;
        const u32 wo0 = (8 * w + kq) * 3456 + ct * 256 + wch * 16;
        const u32 wo1 = wo0 + 4 * 3456;
        auto stage = [&](int st_, u32 dst) {
            const char* xb = x + st_ * 128;
            const char* wb = wqkv + st_ * (64 * 3456);
            dma(xo0, xb, dst + w * 2048);
            dma(wo0, wb, dst + 16384 + w * 2048);
            dma(xo1, xb, dst + w * 2048 + 1024);
            dma(wo1, wb, dst + 16384 + w * 2048 + 1024);
        };
#pragma unroll
        for (int i = 0; i < 3; i++) { stage(s, i * 32768); s = (s == 8) ? 0 : s + 1; }
        const int tg = w & 3, cg = w >> 2;
        const bool active = !(ct == 13 && cg == 1);
        const u32 xa0 = (32 * tg + (l & 15)) * 128 + ((kq ^ ((l & 15) >> 1)) * 16);
        const u32 gsw = q4 | ((kq & 1) << 2);
        u32 wa[4];
#pragma unroll
        for (int c = 0; c < 4; c++) wa[c] = 16384 + (8 * kq + q4) * 256 + (((4 * cg + c) ^ gsw) * 32) + p4 * 8;
        f32x4 acc[2][4];
#pragma unroll
        for (int a = 0; a < 2; a++)
#pragma unroll
            for (int c = 0; c < 4; c++) acc[a][c] = (f32x4){0.f, 0.f, 0.f, 0.f};
#pragma nounroll
        for (int i = 0; i < 9; i++) {
            if (i < 7) WAITV(8);
            else if (moe1) { if (i == 7) WAITV(8); else WAITV(6); }
            else { if (i == 7) WAITV(4); else WAITV(0); }
            BAR();
            if (i < 6) { stage(s, ((i + 3) & 3) * 32768); s = (s == 8) ? 0 : s + 1; }
            else if (moe1) {
                if (i == 6) {
                    dma(gu_off, wg + gu_base, L_GU + w * 1024);
                    dma(gu_off, wg + gu_base + 64 * 9216, L_GU + (w + 8) * 1024);
                    dma(gu_off, wg + gu_base + 128 * 9216, L_GU + (w + 16) * 1024);
                    dma(gu_off, wu + gu_base, L_GU + (w + 24) * 1024);
                } else if (i == 7) {
                    dma(gu_off, wu + gu_base + 64 * 9216, L_GU + (w + 32) * 1024);
                    dma(gu_off, wu + gu_base + 128 * 9216, L_GU + (w + 40) * 1024);
                }
            }
            if (active) {
                const u32 so = (i & 3) * 32768;
                bf16x8 xf[2][2], wf[2][4];
#pragma unroll
                for (int ks = 0; ks < 2; ks++) {
                    const u32 xa = (xa0 ^ (ks * 64)) + so;
                    xf[ks][0] = as8(ld128(xa)); xf[ks][1] = as8(ld128(xa + 2048));
#pragma unroll
                    for (int c = 0; c < 4; c++) {
                        const u32 a = wa[c] + so + ks * 8192;
                        wf[ks][c] = mk8(ldtr(a), ldtr(a + 1024));
                    }
                }
                __builtin_amdgcn_sched_barrier(0);
#pragma unroll
                for (int ks = 0; ks < 2; ks++)
#pragma unroll
                    for (int c = 0; c < 4; c++) {
                        acc[0][c] = MFMA16(wf[ks][c], xf[ks][0], acc[0][c]);
                        acc[1][c] = MFMA16(wf[ks][c], xf[ks][1], acc[1][c]);
                    }
            }
        }
        STAMP(1);
        if (active) {
            const int pb = ct * 32 + cg * 16;
            const float sc = pb < 144 ? QSCALE : 1.f;
            const u32 so = WS_QKV + (pb + kq) * 16384 + (rt * 128 + 32 * tg) * 8 + (l & 15) * 16;
#pragma unroll
            for (int c = 0; c < 4; c++) {
                u32x4 v = {pk2(acc[0][c][0] * sc, acc[0][c][1] * sc), pk2(acc[0][c][2] * sc, acc[0][c][3] * sc),
                           pk2(acc[1][c][0] * sc, acc[1][c][1] * sc), pk2(acc[1][c][2] * sc, acc[1][c][3] * sc)};
                __builtin_amdgcn_raw_buffer_store_b128(v, wsr, so + c * 65536, 0, 16);
            }
        }
        WAITV(0);
        BAR();
        if (tid == 448) __hip_atomic_store((GAS u64*)(ws + WS_QF + (rt * 14 + ct) * 128), TAG, RLX_AGENT);
    } else {
        const int row0 = xcd * 256 + (slot - 28) * 64;
        const GAS char* xp = (const GAS char*)x + (row0 + 8 * w + (l >> 3)) * 1152 + (l & 7) * 16;
        u32x4 xv[9];
#pragma unroll
        for (int i = 0; i < 9; i++) xv[i] = *(const GAS u32x4*)(xp + i * 128);
        float ra = 0.f, ga = 0.f;
#pragma unroll
        for (int i = 0; i < 9; i++) {
            const u32x4 a = *(const GAS u32x4*)((const GAS char*)wr + i * 128 + (l & 7) * 16);
            const u32x4 g = *(const GAS u32x4*)((const GAS char*)gw + i * 128 + (l & 7) * 16);
#pragma unroll
            for (int e = 0; e < 4; e++) {
                const u32 xe = xv[i][e], ae = a[e], ge = g[e];
                ra = __builtin_amdgcn_fdot2_f32_bf16(__builtin_bit_cast(bf16x2, xe), __builtin_bit_cast(bf16x2, ae), ra, false);
                ga = __builtin_amdgcn_fdot2_f32_bf16(__builtin_bit_cast(bf16x2, xe), __builtin_bit_cast(bf16x2, ge), ga, false);
            }
        }
#pragma unroll
        for (int m = 1; m < 8; m <<= 1) { ra += __shfl_xor(ra, m); ga += __shfl_xor(ga, m); }
        u32 rb = __float_as_uint(ra);
        rb = (rb & ~0x7ffu) | ((rb & 0x7ffu) ? 0x800u : 0u);
        u32 key = (rb & 0x80000000u) ? ~rb : (rb | 0x80000000u);
        key = (key & ~0x7ffu) | (2047u - (u32)(row0 + 8 * w + (l >> 3)));
#pragma unroll
        for (int m = 8; m < 64; m <<= 1) key = max(key, (u32)__shfl_xor((int)key, m));
        if ((l & 7) == 0) *(LAS float*)(L_RED8 + (8 * w + (l >> 3)) * 4) = sigm(ga);
        if (l == 0) *(LAS u32*)(L_RED8 + 256 + w * 4) = key;
        BAR();
        if (w == 0) {
            const u32 gv = *(const LAS u32*)(L_RED8 + l * 4);
            __builtin_amdgcn_raw_buffer_store_b32(gv, wsr, WS_G + (row0 + l) * 4, 0, 16);
            u32 k = *(const LAS u32*)(L_RED8 + 256 + (l & 7) * 4);
#pragma unroll
            for (int m = 1; m < 8; m <<= 1) k = max(k, (u32)__shfl_xor((int)k, m));
            WAITV(0);
            if (l == 0) __hip_atomic_store((GAS u64*)(ws + WS_RG + (xcd * 4 + slot - 28) * 8), ((u64)tag32 << 32) | k, RLX_AGENT);
        }
        STAMP(1);
#pragma unroll
        for (int e = 0; e < 6; e++) dma(gu_off, (e < 3 ? wg : wu) + gu_base + (e % 3) * (64 * 9216), L_GU + (w + 8 * e) * 1024);
        WAITV(0);
        BAR();
    }
    STAMP(2);

    const int ai = slot >> 1, aj = xcd * 2 + (slot & 1);
    if (w == 0) {
        u64 v = 0;
#pragma nounroll
        for (int sp = 0; sp < SPIN_CAP; sp++) {
            v = __hip_atomic_load((GAS u64*)(ws + WS_RG + (l & 31) * 8), RLX_AGENT);
            if (__all((u32)(v >> 32) == tag32)) break;
            __builtin_amdgcn_s_sleep(1);
        }
        u32 k = (u32)v;
#pragma unroll
        for (int m = 1; m < 32; m <<= 1) k = max(k, (u32)__shfl_xor((int)k, m));
        k = uni(k);
        const int idx = 2047 - (int)(k & 2047u);
        const u32 kb = k & ~0x7ffu;
        const u32 rb = (kb & 0x80000000u) ? (kb & 0x7fffffffu) : (~kb & ~0x7ffu);
        dma(l * 16, x + idx * 1152, L_XS);
        if (l < 8) dma(l * 16, x + idx * 1152 + 1024, L_XS + 1024);
        if (l == 0) *(LAS u32x2*)L_SEL = (u32x2){(u32)idx, __float_as_uint(bfr(sigm(bfr(__uint_as_float(rb)))))};
        WAITV(0);
    } else if (w == 1) {
        const int ty = l / 6, rr = (l - ty * 6) >> 1;
        const int prt = max(ai - 2 + (ty == 0 ? 2 : rr), 0);
        const int pc = min(ty, 2) * 144 + 9 * aj + ((l & 1) ? 8 : 0);
        const GAS u64* f = (const GAS u64*)(ws + WS_QF + (prt * 14 + (pc >> 5)) * 128);
#pragma nounroll
        for (int sp = 0; sp < SPIN_CAP; sp++) {
            u64 v = TAG;
            if (l < 18) v = __hip_atomic_load(f, RLX_AGENT);
            if (__all(v == TAG)) break;
            __builtin_amdgcn_s_sleep(1);
        }
    } else if (w == 2) {
        if (l < 32) *(LAS u32x2*)(L_ONE + l * 8) = (u32x2){0x00003f80u, 0u};
    }
    BAR();
    STAMP(3);
#pragma unroll
    for (int e = 0; e < 8; e++) {
        const int u = min(w + 8 * e, 62);
        int pc, prt;
        u32 dst;
        if (u < 9) { pc = u; prt = ai; dst = L_Q + u * 1024; }
        else {
            const int v = u - 9, rr = v / 18, z = v - rr * 18;
            pc = (z < 9) ? 144 + z : 288 + z - 9;
            prt = max(ai - 2 + rr, 0);
            dst = L_K + z * PSTR + rr * 1024;
        }
        dma_sc1(l * 16, ws + WS_QKV + (pc + 9 * aj) * 16384 + prt * 1024, dst);
    }
    if (moe1) {
        const int m = w >> 2;
        const u32 a0 = L_GU + m * 24576 + (8 * kq + q4) * 128 + (((w & 3) ^ ((q4 >> 1) | ((kq & 1) << 1))) * 32) + p4 * 8;
        const u32 xs = L_XS + (third * 192 + 8 * kq) * 2;
        f32x4 d = {0.f, 0.f, 0.f, 0.f};
#pragma unroll
        for (int ks = 0; ks < 6; ks++) d = MFMA16(as8(ld128(xs + ks * 64)), mk8(ldtr(a0 + ks * 4096), ldtr(a0 + ks * 4096 + 512)), d);
        if (l < 16)
            __hip_atomic_store((GAS u64*)(ws + WS_HP + ((m1 * 2 + m) * 64 + (w & 3) * 16 + l) * 8), ((u64)tag32 << 32) | __float_as_uint(d[0]), RLX_AGENT);
    }
    BAR();
    const int oc = slot & 7, orow = xcd * 256 + (slot >> 3) * 64;
#pragma unroll
    for (int e = 0; e < 12; e++) {
        const int u = w + 8 * e;
        if (e < 10 || (e == 10 && w == 0)) {
            const u32 id = u * 64 + l, row = (id * 7282u) >> 16;
            dma(row * 1008 + id * 16, wo + oc * 144, L_WO + u * 1024);
        } else {
            const int z = min(u - 81, 11);
            dma(l * 16, wd + b * 20736 + z * 1024, L_D1 + z * 1024);
        }
    }
    const int hh = w >> 2, t = hh ? 3 - (w & 3) : (w & 3), h = l >> 5, pos = l & 31, pm = hh ? 5 : 0;
    f32x16 mdiag, mold;
    {
        const float tq = (float)((pos >> 1) + 16 * (pos & 1)) + 0.5f - (float)(2 * h);
#pragma unroll
        for (int r = 0; r < 16; r++) {
            const float uu = (tq - (float)(((r & 3) >> 1) + 4 * (r >> 2) + 16 * (r & 1))) * 1e30f;
            mdiag[r] = fminf(uu, 0.f);
            mold[r] = fminf(-uu, 0.f);
        }
    }
    WAITV(12);
    BAR();
    STAMP(4);
    bf16x8 qb1;
    u32x4 qb2;
    {
        const u32 qa = L_Q + (32 * t + pos) * 8;
        qb1 = mk8(ld64(qa + (pm + 2 * h) * 1024), ld64(qa + (pm + 2 * h + 1) * 1024));
        const u32x2 z = ld64(qa + 4096);
        qb2 = (u32x4){(h | hh) ? 0u : z.x, (h == 0 && hh) ? z.y : 0u, 0u, h ? 0u : 0xff7f0000u};
    }
    BAR();
    dma(l * 16, wd + b * 20736 + 11520 + w * 1024, L_Q + w * 1024);
    if (w == 0) dma(l * 16, wd + b * 20736 + 11520 + 8192, L_Q + 8192);

    f32x16 o;
    {
        const u32 ka = L_K + (pm + 2 * h) * PSTR + (32 * t + pos) * 8;
        const u32 kb = L_K + 4 * PSTR + (32 * t + pos) * 8;
        const bool cst = (l & 16) && p4;
        const u32 va = cst ? L_ONE + (4 * h + q4) * 8 : L_V + ((l & 16) ? 4 : pm + p4) * PSTR + (32 * t + 4 * h + q4) * 8;
        const u32 vstep = cst ? 0u : 256u;
        const int nfirst = 8 - 4 * ai - t;
        const f32x16 zero = {0.f, 0.f, 0.f, 0.f, 0.f, 0.f, 0.f, 0.f, 0.f, 0.f, 0.f, 0.f, 0.f, 0.f, 0.f, 0.f};
        auto scores = [&](int n, const f32x16& c) {
            const bf16x8 k1 = mk8(ld64(ka + n * 256), ld64(ka + n * 256 + PSTR));
            const u32x2 z = ld64(kb + n * 256);
            const bf16x8 k2 = as8((u32x4){z.x, z.y, 0x3f803f80u, n < nfirst ? 0x3f803f80u : 0x00003f80u});
            return MFMA32(k2, as8(qb2), MFMA32(k1, qb1, c));
        };
#pragma nounroll
        for (int attempt = 0; attempt < 2; attempt++) {
            o = zero;
            f32x16 sc = scores(0, mold);
#pragma unroll
            for (int n = 0; n < 9; n++) {
                f32x16 sn;
                if (n < 8) sn = scores(n + 1, n == 7 ? mdiag : zero);
                const u32 v0 = va + n * vstep;
                const bf16x8 vf0 = mk8(ldtr(v0), ldtr(v0 + 64)), vf1 = mk8(ldtr(v0 + 128), ldtr(v0 + 192));
                u32 p[8];
#pragma unroll
                for (int r = 0; r < 8; r++) p[r] = pk2(__builtin_amdgcn_exp2f(sc[2 * r]), __builtin_amdgcn_exp2f(sc[2 * r + 1]));
                o = MFMA32(vf0, as8((u32x4){p[0], p[1], p[2], p[3]}), o);
                o = MFMA32(vf1, as8((u32x4){p[4], p[5], p[6], p[7]}), o);
                if (n < 8) sc = sn;
            }
            if (attempt) break;
            const float lsum = h ? o[8] : o[12];
            float mx = 0.f;
#pragma unroll
            for (int r = 0; r < 12; r++) mx = fmaxf(mx, fabsf(o[r]));
            const bool ok = lsum >= 0x1p-40f && lsum <= 0x1p60f && mx <= 0x1p120f;
            if (__all(ok)) break;
            float m = -3e38f;
#pragma nounroll
            for (int n = 0; n < 9; n++) {
                f32x16 c = zero;
                if (n == 0) c = mold;
                if (n == 8) c = mdiag;
                const f32x16 s2 = scores(n, c);
#pragma unroll
                for (int r = 0; r < 16; r++) m = fmaxf(m, s2[r]);
            }
            m = fmaxf(m, __shfl_xor(m, 32));
            const u32 mh = __float_as_uint(-m) & 0xffff0000u;
            const float r1 = -m - __uint_as_float(mh);
            const u32 mm = __float_as_uint(r1) & 0xffff0000u;
            const u32 ml = pk2(r1 - __uint_as_float(mm), 0.f) & 0xffffu;
            if (h == 0) { qb2.z = (mh >> 16) | mm; qb2.w = 0xff7f0000u | ml; }
        }
        const float li = __builtin_amdgcn_rcpf(h ? o[8] : o[12]);
        const u32 prow = 128 * ai + 32 * t + pos;
        const u32x4 v = {pk2(o[0] * li, o[1] * li), pk2(o[2] * li, o[3] * li), pk2(o[4] * li, o[5] * li), pk2(o[6] * li, o[7] * li)};
        __builtin_amdgcn_raw_buffer_store_b128(v, wsr, WS_O + ((4 * aj + 2 * hh + h) * 2048 + prow) * 16, 0, 16);
        const u32 z = hh ? pk2(o[10] * li, o[11] * li) : pk2(o[8] * li, o[9] * li);
        if (h == 0) __builtin_amdgcn_raw_buffer_store_b32(z, wsr, WS_OP + ((2 * aj + hh) * 2048 + prow) * 4, 0, 16);
    }
    STAMP(5);
    WAITV(0);
    BAR();
    if (tid == 448) __hip_atomic_store((GAS u64*)(ws + WS_OF + (ai * 16 + aj) * 128), TAG, RLX_AGENT);
    STAMP(6);

    if (w == 0) {
        const int ff = 18 * b + min(l, 17);
        const GAS u64* hp = (const GAS u64*)(ws + WS_HP + ((ff >> 6) * 384 + (ff & 63)) * 8);
        u64 v[6];
#pragma nounroll
        for (int sp = 0; sp < SPIN_CAP; sp++) {
            bool ok = true;
#pragma unroll
            for (int e = 0; e < 6; e++) { v[e] = __hip_atomic_load(hp + e * 64, RLX_AGENT); ok &= (u32)(v[e] >> 32) == tag32; }
            if (__all(ok)) break;
            __builtin_amdgcn_s_sleep(1);
        }
        const float gg = bfr(__uint_as_float((u32)v[0]) + __uint_as_float((u32)v[2]) + __uint_as_float((u32)v[4]));
        const float uu = bfr(__uint_as_float((u32)v[1]) + __uint_as_float((u32)v[3]) + __uint_as_float((u32)v[5]));
        const u32 hv = pk2(bfr(gg * sigm(gg)) * uu, 0.f);
        if (l < 32) *(LAS u16*)(L_H + l * 2) = l < 18 ? (u16)hv : (u16)0;
    }
    BAR();
    {
        const bf16x8 hb = as8(ld128(L_H + kq * 16));
        u32 ra[2];
#pragma unroll
        for (int z = 0; z < 2; z++) {
            const int k = 8 * kq + 4 * z + q4;
            ra[z] = (k < 10 ? L_D1 + k * 1152 : k < 18 ? L_Q + (k - 10) * 1152 : L_D1) + p4 * 8 + w * 32;
        }
#pragma unroll
        for (int e = 0; e < 5; e++) {
            if (e < 4 || w < 4) {
                const f32x4 d = MFMA16(mk8(ldtr(ra[0] + e * 256), ldtr(ra[1] + e * 256)), hb, ((f32x4){0.f, 0.f, 0.f, 0.f}));
                if ((l & 15) == 0) *(LAS u32x2*)(L_XS + (w + 8 * e) * 32 + kq * 8) = (u32x2){pk2(d[0], d[1]), pk2(d[2], d[3])};
            }
        }
    }
    BAR();
    if (w == 0) {
#pragma unroll
        for (int z = 0; z < 2; z++) {
            const u32 id = l + 64 * z, c = (id * 7282u) >> 16;
            if (id < 72) __builtin_amdgcn_raw_buffer_store_b128(ld128(L_XS + id * 16), wsr, WS_YP + c * (36864 - 144) + b * 144 + id * 16, 0, 16);
        }
        WAITV(0);
        if (l == 0) __hip_atomic_store((GAS u64*)(ws + WS_YF + b * 8), TAG, RLX_AGENT);
    } else if (w == 1) {
        const GAS u64* f = (const GAS u64*)(ws + WS_OF + ((orow >> 7) * 16 + (l & 15)) * 128);
#pragma nounroll
        for (int sp = 0; sp < SPIN_CAP; sp++) {
            u64 v = TAG;
            if (l < 16) v = __hip_atomic_load(f, RLX_AGENT);
            if (__all(v == TAG)) break;
            __builtin_amdgcn_s_sleep(1);
        }
    }
    u32x4 xm[2];
    float gm[2];
    u32 tok[2], rho[2], chn[2];
#pragma unroll
    for (int z = 0; z < 2; z++) {
        const u32 id = min(tid + 512 * z, 575);
        rho[z] = (id * 7282u) >> 16;
        chn[z] = id - rho[z] * 9;
        tok[z] = orow + (rho[z] & 32) + ((rho[z] & 31) >> 1) + 16 * (rho[z] & 1);
        if (z == 0 || w == 0) {
            xm[z] = *(const GAS u32x4*)((const GAS char*)x + tok[z] * 1152 + oc * 144 + chn[z] * 16);
            gm[z] = __uint_as_float(__builtin_amdgcn_raw_buffer_load_b32(wsr, WS_G + tok[z] * 4, 0, 16));
        }
    }
    BAR();
    STAMP(7);

    {
        const int tt = w & 3, kh = w >> 2;
        const u32 prow = orow + 16 * tt + (l & 15);
        u32x4 ob[9];
        const u32 oa = WS_O + ((36 * kh + kq) * 2048 + prow) * 16;
#pragma unroll
        for (int n = 0; n < 7; n++) ob[n] = __builtin_amdgcn_raw_buffer_load_b128(wsr, oa, n * 131072, 16);
        if (kh == 0) {
#pragma unroll
            for (int n = 7; n < 9; n++) ob[n] = __builtin_amdgcn_raw_buffer_load_b128(wsr, oa, n * 131072, 16);
        } else {
#pragma unroll
            for (int n = 7; n < 9; n++)
#pragma unroll
                for (int e = 0; e < 4; e++)
                    ob[n][e] = __builtin_amdgcn_raw_buffer_load_b32(wsr, WS_OP + (4 * kq * 2048 + prow) * 4, (16 * (n - 7) + e) * 8192, 16);
        }
        const u32 am = L_WO + (324 * kh + 20 * (kq >> 1) + 4 * (kq & 1) + q4) * 144 + p4 * 8;
        const u32 ap = L_WO + (72 * kq + 16 + q4) * 144 + p4 * 8;
        f32x4 acc[5];
#pragma unroll
        for (int c = 0; c < 5; c++) acc[c] = (f32x4){0.f, 0.f, 0.f, 0.f};
#pragma unroll
        for (int n = 0; n < 9; n++) {
            const bool tail = n >= 7 && kh;
            const u32 a0 = tail ? ap + (n - 7) * (288 * 144) : am + n * (36 * 144);
            const u32 a1 = a0 + (tail ? 36 * 144 : 8 * 144);
#pragma unroll
            for (int c = 0; c < 5; c++) acc[c] = MFMA16(mk8(ldtr(a0 + c * 32), ldtr(a1 + c * 32)), as8(ob[n]), acc[c]);
        }
#pragma unroll
        for (int c = 0; c < 5; c++) *(LAS f32x4*)(L_RED + kh * 21504 + (16 * tt + (l & 15)) * 336 + c * 64 + kq * 16) = acc[c];
    }
    BAR();
    STAMP(8);
    const u32x2 sel = ld64(L_SEL);
    const int idx = uni(sel.x);
    const bool fin = (idx >> 6) == (orow >> 6);
    if (fin) {
        if (w == 0) {
#pragma nounroll
        for (int sp = 0; sp < SPIN_CAP; sp++) {
                bool ok = true;
#pragma unroll
                for (int e = 0; e < 4; e++) ok &= __hip_atomic_load((GAS u64*)(ws + WS_YF + (l + 64 * e) * 8), RLX_AGENT) == TAG;
                if (__all(ok)) break;
                __builtin_amdgcn_s_sleep(1);
            }
            if (l == 0 && oc == 0) __hip_atomic_store((GAS u64*)(ws + WS_EPOCH), TAG, RLX_AGENT);
        }
        BAR();
#pragma unroll
        for (int e = 0; e < 5; e++)
            if (e < 4 || w < 4) dma_sc1(l * 16, ws + WS_YP + oc * 36864 + (w + 8 * e) * 1024, L_WO + (w + 8 * e) * 1024);
        WAITV(0);
        BAR();
        const u32 a0 = L_WO + (32 * w + 8 * kq + q4) * 144 + p4 * 8;
        const bf16x8 ones = as8((u32x4){0x3f803f80u, 0x3f803f80u, 0x3f803f80u, 0x3f803f80u});
#pragma unroll
        for (int c = 0; c < 5; c++) {
            const f32x4 d = MFMA16(mk8(ldtr(a0 + c * 32), ldtr(a0 + 4 * 144 + c * 32)), ones, ((f32x4){0.f, 0.f, 0.f, 0.f}));
            if ((l & 15) == 0) *(LAS f32x4*)(L_YR + w * 320 + c * 64 + kq * 16) = d;
        }
        BAR();
    }
#pragma unroll
    for (int z = 0; z < 2; z++) {
        if (z == 0 || w == 0) {
            const u32 a = L_RED + rho[z] * 336 + chn[z] * 32;
            const f32x4 a0 = *(const LAS f32x4*)a + *(const LAS f32x4*)(a + 21504);
            const f32x4 a1 = *(const LAS f32x4*)(a + 16) + *(const LAS f32x4*)(a + 21504 + 16);
            float bv[8];
#pragma unroll
            for (int e = 0; e < 4; e++) { bv[2 * e] = lo16(xm[z][e]); bv[2 * e + 1] = hi16(xm[z][e]); }
            if (fin && (int)tok[z] == idx) {
                f32x4 y0 = {0.f, 0.f, 0.f, 0.f}, y1 = y0;
#pragma unroll
                for (int k = 0; k < 8; k++) {
                    y0 += *(const LAS f32x4*)(L_YR + k * 320 + chn[z] * 32);
                    y1 += *(const LAS f32x4*)(L_YR + k * 320 + chn[z] * 32 + 16);
                }
                const float rs = __uint_as_float(sel.y);
#pragma unroll
                for (int e = 0; e < 4; e++) { bv[e] += bfr(y0[e]) * rs; bv[4 + e] += bfr(y1[e]) * rs; }
            }
            const float g = gm[z], g1 = 1.f - g;
            const u32x4 v = {pk2(g * a0[0] + g1 * bv[0], g * a0[1] + g1 * bv[1]), pk2(g * a0[2] + g1 * bv[2], g * a0[3] + g1 * bv[3]),
                             pk2(g * a1[0] + g1 * bv[4], g * a1[1] + g1 * bv[5]), pk2(g * a1[2] + g1 * bv[6], g * a1[3] + g1 * bv[7])};
            *(GAS u32x4*)((GAS char*)out + tok[z] * 1152 + oc * 144 + chn[z] * 16) = v;
        }
    }
    STAMP(9);
    WAITV(0);
    STAMP(10);
    { u64 c1; asm volatile("s_memtime %0\n\ts_waitcnt lgkmcnt(0)" : "=s"(c1)::"memory"); if (b == 0 && tid == 0) { ((GAS u64*)(ws + WS_DBG))[8192] = c1 - c0; ((GAS u64*)(ws + WS_DBG))[8193] = st[10] - st[0]; } }
    if (w == 0) {
        u64 c1; asm volatile("s_memtime %0\n\ts_waitcnt lgkmcnt(0)" : "=s"(c1)::"memory");
        u64 ta = 0;
        if (AMP == 17) ta = __hip_atomic_load((GAS u64*)(ws + WS_DBG + 65536 + 8 * 64), RLX_AGENT);
        if (tid == 0) ((GAS u64*)(ws + WS_DBG))[b] = st[10];
        u64 tgt = AMP == 17 ? (st[0] - ta) * 10 : AMP < 16 ? (st[AMP & 15] - st[0]) * 10 : (u64)((float)(c1 - c0) / (float)(st[10] - st[0]) * (10000.f / 24.f));
        if (tgt > 100000) tgt = 100000;
        while (__builtin_amdgcn_s_memrealtime() - st[10] < tgt) __builtin_amdgcn_s_sleep(4);
    }
}

__global__ void tinyA(char* ws) { if (threadIdx.x == 0) ((GAS u64*)(ws + WS_DBG + 65536 + 8 * 64))[0] = __builtin_amdgcn_s_memrealtime(); }
__global__ void tinyC(char* ws, int amp) { u64 t = __builtin_amdgcn_s_memrealtime(); u64 m = 0; for (int i = 0; i < 4; i++) { u64 v = ((GAS u64*)(ws + WS_DBG))[threadIdx.x + 64 * i]; m = v > m ? v : m; }
  for (int k = 1; k < 64; k <<= 1) { u64 o = __shfl_xor(m, k); m = o > m ? o : m; }
  u64 tgt = (t - m) * amp; if (tgt > 100000) tgt = 100000; while (__builtin_amdgcn_s_memrealtime() - t < tgt) __builtin_amdgcn_s_sleep(4); }
extern "C" void kernel_launch(void* const* d_in, const int* in_sizes, int n_in, void* d_out, int out_size, void* d_ws, size_t ws_size,
                              hipStream_t stream) {
    (void)hipFuncSetAttribute((const void*)fused_kernel, hipFuncAttributeMaxDynamicSharedMemorySize, L_TOTAL);
    fused_kernel<<<256, 512, L_TOTAL, stream>>>((const char*)d_in[0], (const char*)d_in[1], (const char*)d_in[2], (const char*)d_in[4],
                                                (const char*)d_in[5], (const char*)d_in[6], (const char*)d_in[7], (const char*)d_in[8],
                                                (char*)d_out, (char*)d_ws);
}
```

```cpp
#include <hip/hip_runtime.h>

typedef unsigned int u32;

__global__ void __launch_bounds__(512, 2)
fused_kernel(const void* x, u32 xn, u32 xf, const void* wqkv, u32 qn, u32 qf, void* ws, u32 sn, u32 sf, const void* wg, u32 gn, u32 gf,
             const void* wu, u32 un, u32 uf, const void* wd, u32 dn, u32 df, const void* wo, u32 on, u32 of, void* out, u32 tn, u32 tf,
             const void* wr, const void* gw) {
    unsigned long long ka = (unsigned long long)__builtin_amdgcn_kernarg_segment_ptr();
    u32 bid = __builtin_amdgcn_workgroup_id_x();
    u32 tid = __builtin_amdgcn_workitem_id_x();
    asm volatile(
        ".Lmk_start:\n"
        "s_load_dwordx16 s[4:19], s[0:1], 0x0\n"
        "s_load_dwordx16 s[20:35], s[0:1], 0x40\n"
        "s_load_dwordx4 s[36:39], s[0:1], 0x80\n"
        "v_readfirstlane_b32 s42, v0\n"
        "s_nop 0\n"
        "s_lshr_b32 s42, s42, 6\n"
        "s_and_b32 s43, s2, 7\n"
        "s_lshr_b32 s44, s2, 3\n"
        "s_and_b32 s45, s42, 3\n"
        "s_lshr_b32 s46, s42, 2\n"
        "s_xor_b32 s46, s46, 1\n"
        "v_and_b32 v0, 63, v0\n"
        "v_lshrrev_b32 v14, 4, v0\n"
        "v_and_b32 v15, 15, v0\n"
        "s_lshl_b32 s55, s45, 11\n"
        "v_lshrrev_b32 v248, 3, v0\n"
        "s_cmp_ge_u32 s44, 28\n"
        "s_cbranch_scc1 .Lmk_router\n"
        "s_mul_i32 s98, s44, 37\n"
        "s_lshr_b32 s98, s98, 8\n"
        "s_mul_i32 s99, s98, 7\n"
        "s_sub_u32 s99, s44, s99\n"
        "s_lshr_b32 s92, s43, 1\n"
        "s_and_b32 s94, s43, 1\n"
        "s_mul_i32 s49, s99, 3\n"
        "s_mul_i32 s95, s98, 3\n"
        "s_add_u32 s49, s49, s95\n"
        "s_mul_i32 s95, s92, 1\n"
        "s_add_u32 s49, s49, s95\n"
        "s_mul_i32 s95, s94, 0\n"
        "s_add_u32 s49, s49, s95\n"
        "s_mul_i32 s95, s49, 57\n"
        "s_lshr_b32 s95, s95, 9\n"
        "s_mul_i32 s95, s95, 9\n"
        "s_sub_u32 s49, s49, s95\n"
        "s_lshl2_add_u32 s98, s92, s98\n"
        "s_mul_i32 s94, s94, 7\n"
        "s_add_u32 s99, s99, s94\n"
        "s_lshr_b32 s93, s98, 1\n"
        "s_lshl1_add_u32 s93, s93, s98\n"
        "s_cmp_eq_u32 s46, 0\n"
        "s_cbranch_scc0 .Lmk_comp\n"
        "s_setprio 3\n"
        "s_lshl2_add_u32 s92, s93, s45\n"
        "v_lshl_add_u32 v249, s92, 4, v248\n"
        "v_mul_u32_u24 v249, 1152, v249\n"
        "v_and_b32 v251, 7, v0\n"
        "v_xor_b32 v251, v251, v14\n"
        "v_lshl_add_u32 v40, v251, 4, v249\n"
        "v_xor_b32 v41, 64, v40\n"
        "v_add_u32 v41, 8192, v41\n"
        "s_and_b32 s94, s45, 1\n"
        "s_lshl_b32 s94, s94, 6\n"
        "s_mul_i32 s95, s45, 9216\n"
        "v_xor_b32 v46, s94, v40\n"
        "v_subrev_u32 v46, s95, v46\n"
        "v_lshlrev_b32 v250, 1, v14\n"
        "v_xor_b32 v250, v250, v15\n"
        "v_xor_b32 v252, 8, v250\n"
        "s_cmp_eq_u32 s99, 13\n"
        "s_cselect_b32 s94, 7, 15\n"
        "v_and_b32 v250, s94, v250\n"
        "v_and_b32 v252, s94, v252\n"
        "v_lshl_add_u32 v251, s45, 4, v14\n"
        "v_mul_u32_u24 v251, 3456, v251\n"
        "v_lshl_add_u32 v251, s99, 8, v251\n"
        "v_lshl_add_u32 v42, v250, 4, v251\n"
        "v_lshl_add_u32 v44, v252, 4, v251\n"
        "v_add_u32 v43, 12800, v42\n"
        "v_add_u32 v45, 38400, v44\n"
        "v_add_u32 v44, 25600, v44\n"
        "s_lshl_b32 s47, s49, 7\n"
        "s_mul_i32 s48, s49, 221184\n"
        "s_add_u32 s50, s55, 57344\n"
        "s_lshl1_add_u32 s51, s55, 65536\n"
        "s_lshl_b32 s49, s49, 7\n"
        "s_lshl_b32 s52, s45, 6\n"
        "s_waitcnt lgkmcnt(0)\n"
        "s_add_u32 m0, s51, 0\n"
        "s_nop 0\n"
        "buffer_load_dwordx4 v42, s[8:11], s48 offen lds\n"
        "buffer_load_dwordx4 v43, s[8:11], s48 offen offset:1024 lds\n"
        "buffer_load_dwordx4 v44, s[8:11], s48 offen offset:2048 lds\n"
        "buffer_load_dwordx4 v45, s[8:11], s48 offen offset:3072 lds\n"
        "s_add_u32 m0, s50, 0\n"
        "s_nop 0\n"
        "buffer_load_dwordx4 v40, s[4:7], s47 offen lds\n"
        "buffer_load_dwordx4 v41, s[4:7], s47 offen offset:1024 lds\n"
        "s_add_u32 s47, s47, 128\n"
        "s_add_u32 s48, s48, 221184\n"
        "s_cmp_eq_u32 s47, 1152\n"
        "s_cselect_b32 s47, 0, s47\n"
        "s_cselect_b32 s48, 0, s48\n"
        "s_load_dwordx2 s[40:41], s[12:13], 0x0\n"
        "s_barrier\n"
        "s_add_u32 m0, s51, 24576\n"
        "s_nop 0\n"
        "buffer_load_dwordx4 v42, s[8:11], s48 offen lds\n"
        "buffer_load_dwordx4 v43, s[8:11], s48 offen offset:1024 lds\n"
        "buffer_load_dwordx4 v44, s[8:11], s48 offen offset:2048 lds\n"
        "buffer_load_dwordx4 v45, s[8:11], s48 offen offset:3072 lds\n"
        "s_add_u32 m0, s50, 24576\n"
        "s_nop 0\n"
        "buffer_load_dwordx4 v40, s[4:7], s47 offen lds\n"
        "buffer_load_dwordx4 v41, s[4:7], s47 offen offset:1024 lds\n"
        "s_add_u32 s47, s47, 128\n"
        "s_add_u32 s48, s48, 221184\n"
        "s_cmp_eq_u32 s47, 1152\n"
        "s_cselect_b32 s47, 0, s47\n"
        "s_cselect_b32 s48, 0, s48\n"
        "s_add_u32 m0, s51, 49152\n"
        "s_nop 0\n"
        "buffer_load_dwordx4 v42, s[8:11], s48 offen lds\n"
        "buffer_load_dwordx4 v43, s[8:11], s48 offen offset:1024 lds\n"
        "buffer_load_dwordx4 v44, s[8:11], s48 offen offset:2048 lds\n"
        "buffer_load_dwordx4 v45, s[8:11], s48 offen offset:3072 lds\n"
        "s_add_u32 m0, s50, 49152\n"
        "s_nop 0\n"
        "buffer_load_dwordx4 v40, s[4:7], s47 offen lds\n"
        "buffer_load_dwordx4 v41, s[4:7], s47 offen offset:1024 lds\n"
        "s_add_u32 s47, s47, 128\n"
        "s_add_u32 s48, s48, 221184\n"
        "s_cmp_eq_u32 s47, 1152\n"
        "s_cselect_b32 s47, 0, s47\n"
        "s_cselect_b32 s48, 0, s48\n"
        "s_setprio 0\n"
        "v_lshl_add_u32 v249, s42, 6, v0\n"
        "v_subrev_u32 v248, 280, v249\n"
        "v_mov_b32 v250, 0x3f80\n"
        "v_mov_b32 v251, 0\n"
        "v_lshlrev_b32 v252, 3, v249\n"
        "v_ashrrev_i32 v248, 31, v248\n"
        "v_bfi_b32 v250, v248, 0, v250\n"
        "v_add_u32 v252, 155648, v252\n"
        "ds_write_b64 v252, v[250:251]\n"
        "v_and_b32 v254, 0x3f80, v248\n"
        "v_mov_b32 v255, 0\n"
        "v_min_u32 v252, 159480, v252\n"
        "ds_write_b64 v252, v[254:255] offset:4096\n"
        "s_mul_i32 s98, s43, 27\n"
        "s_add_u32 s98, s98, s44\n"
        "s_sub_u32 s98, s98, 1\n"
        "s_mul_i32 s99, s98, 171\n"
        "s_lshr_b32 s99, s99, 9\n"
        "s_mul_i32 s92, s99, 3\n"
        "s_sub_u32 s92, s98, s92\n"
        "s_mul_i32 s97, s92, 384\n"
        "s_lshl_b32 s96, s98, 10\n"
        "s_mul_i32 s92, s92, 12\n"
        "s_add_u32 s92, s92, s45\n"
        "s_mul_i32 s92, s92, 147456\n"
        "s_lshl_b32 s99, s99, 7\n"
        "s_add_u32 s54, s92, s99\n"
        "v_lshrrev_b32 v248, 3, v0\n"
        "v_and_b32 v249, 1, v14\n"
        "v_bfe_u32 v250, v0, 1, 2\n"
        "v_xor_b32 v249, v249, v250\n"
        "v_and_b32 v250, 1, v0\n"
        "v_lshl_or_b32 v249, v249, 1, v250\n"
        "v_mul_u32_u24 v250, 9216, v248\n"
        "s_sub_u32 s94, s44, 1\n"
        "s_cmp_ge_u32 s94, 27\n"
        "s_cselect_b32 s94, 2147483648, 0\n"
        "s_cselect_b32 s54, 0, s54\n"
        "s_cselect_b32 s97, 0, s97\n"
        "s_cselect_b32 s96, 2147483648, s96\n"
        "v_lshl_add_u32 v1, v249, 4, v250\n"
        "v_or_b32 v1, s94, v1\n"
        "v_xor_b32 v2, 64, v1\n"
        "v_add_u32 v2, 72704, v2\n"
        "s_waitcnt lgkmcnt(0)\n"
        "s_mul_i32 s98, s40, 0x5851f42d\n"
        "s_mul_hi_u32 s99, s40, 0x4c957f2d\n"
        "s_mul_i32 s92, s41, 0x4c957f2d\n"
        "s_add_u32 s98, s98, s99\n"
        "s_add_u32 s98, s98, s92\n"
        "s_mul_i32 s40, s40, 0x4c957f2d\n"
        "s_add_u32 s40, s40, 0xf767814f\n"
        "s_addc_u32 s41, s98, 0x14057b7e\n"
        "s_waitcnt vmcnt(12)\n"
        "s_barrier\n"
        "s_add_u32 m0, s51, 73728\n"
        "s_nop 0\n"
        "buffer_load_dwordx4 v42, s[8:11], s48 offen lds\n"
        "buffer_load_dwordx4 v43, s[8:11], s48 offen offset:1024 lds\n"
        "buffer_load_dwordx4 v44, s[8:11], s48 offen offset:2048 lds\n"
        "buffer_load_dwordx4 v45, s[8:11], s48 offen offset:3072 lds\n"
        "s_add_u32 m0, s50, 73728\n"
        "s_nop 0\n"
        "buffer_load_dwordx4 v40, s[4:7], s47 offen lds\n"
        "buffer_load_dwordx4 v41, s[4:7], s47 offen offset:1024 lds\n"
        "s_add_u32 s47, s47, 128\n"
        "s_add_u32 s48, s48, 221184\n"
        "s_cmp_eq_u32 s47, 1152\n"
        "s_cselect_b32 s47, 0, s47\n"
        "s_cselect_b32 s48, 0, s48\n"
        "s_waitcnt vmcnt(12)\n"
        "s_barrier\n"
        "s_add_u32 m0, s51, 0\n"
        "s_nop 0\n"
        "buffer_load_dwordx4 v42, s[8:11], s48 offen lds\n"
        "buffer_load_dwordx4 v43, s[8:11], s48 offen offset:1024 lds\n"
        "buffer_load_dwordx4 v44, s[8:11], s48 offen offset:2048 lds\n"
        "buffer_load_dwordx4 v45, s[8:11], s48 offen offset:3072 lds\n"
        "s_add_u32 m0, s50, 0\n"
        "s_nop 0\n"
        "buffer_load_dwordx4 v40, s[4:7], s47 offen lds\n"
        "buffer_load_dwordx4 v41, s[4:7], s47 offen offset:1024 lds\n"
        "s_add_u32 s47, s47, 128\n"
        "s_add_u32 s48, s48, 221184\n"
        "s_cmp_eq_u32 s47, 1152\n"
        "s_cselect_b32 s47, 0, s47\n"
        "s_cselect_b32 s48, 0, s48\n"
        "s_waitcnt vmcnt(12)\n"
        "s_barrier\n"
        "s_add_u32 m0, s51, 24576\n"
        "s_nop 0\n"
        "buffer_load_dwordx4 v42, s[8:11], s48 offen lds\n"
        "buffer_load_dwordx4 v43, s[8:11], s48 offen offset:1024 lds\n"
        "buffer_load_dwordx4 v44, s[8:11], s48 offen offset:2048 lds\n"
        "buffer_load_dwordx4 v45, s[8:11], s48 offen offset:3072 lds\n"
        "s_add_u32 m0, s50, 24576\n"
        "s_nop 0\n"
        "buffer_load_dwordx4 v40, s[4:7], s47 offen lds\n"
        "buffer_load_dwordx4 v41, s[4:7], s47 offen offset:1024 lds\n"
        "s_add_u32 s47, s47, 128\n"
        "s_add_u32 s48, s48, 221184\n"
        "s_cmp_eq_u32 s47, 1152\n"
        "s_cselect_b32 s47, 0, s47\n"
        "s_cselect_b32 s48, 0, s48\n"
        "s_waitcnt vmcnt(12)\n"
        "s_barrier\n"
        "s_add_u32 m0, s51, 49152\n"
        "s_nop 0\n"
        "buffer_load_dwordx4 v42, s[8:11], s48 offen lds\n"
        "buffer_load_dwordx4 v43, s[8:11], s48 offen offset:1024 lds\n"
        "buffer_load_dwordx4 v44, s[8:11], s48 offen offset:2048 lds\n"
        "buffer_load_dwordx4 v45, s[8:11], s48 offen offset:3072 lds\n"
        "s_add_u32 m0, s50, 49152\n"
        "s_nop 0\n"
        "buffer_load_dwordx4 v40, s[4:7], s47 offen lds\n"
        "buffer_load_dwordx4 v41, s[4:7], s47 offen offset:1024 lds\n"
        "s_add_u32 s47, s47, 128\n"
        "s_add_u32 s48, s48, 221184\n"
        "s_cmp_eq_u32 s47, 1152\n"
        "s_cselect_b32 s47, 0, s47\n"
        "s_cselect_b32 s48, 0, s48\n"
        "s_waitcnt vmcnt(12)\n"
        "s_barrier\n"
        "s_add_u32 m0, s51, 73728\n"
        "s_nop 0\n"
        "buffer_load_dwordx4 v42, s[8:11], s48 offen lds\n"
        "buffer_load_dwordx4 v43, s[8:11], s48 offen offset:1024 lds\n"
        "buffer_load_dwordx4 v44, s[8:11], s48 offen offset:2048 lds\n"
        "buffer_load_dwordx4 v45, s[8:11], s48 offen offset:3072 lds\n"
        "s_add_u32 m0, s50, 73728\n"
        "s_nop 0\n"
        "buffer_load_dwordx4 v40, s[4:7], s47 offen lds\n"
        "buffer_load_dwordx4 v41, s[4:7], s47 offen offset:1024 lds\n"
        "s_add_u32 s47, s47, 128\n"
        "s_add_u32 s48, s48, 221184\n"
        "s_cmp_eq_u32 s47, 1152\n"
        "s_cselect_b32 s47, 0, s47\n"
        "s_cselect_b32 s48, 0, s48\n"
        "s_waitcnt vmcnt(12)\n"
        "s_barrier\n"
        "s_add_u32 m0, s51, 0\n"
        "s_nop 0\n"
        "buffer_load_dwordx4 v42, s[8:11], s48 offen lds\n"
        "buffer_load_dwordx4 v43, s[8:11], s48 offen offset:1024 lds\n"
        "buffer_load_dwordx4 v44, s[8:11], s48 offen offset:2048 lds\n"
        "buffer_load_dwordx4 v45, s[8:11], s48 offen offset:3072 lds\n"
        "s_add_u32 m0, s50, 0\n"
        "s_nop 0\n"
        "buffer_load_dwordx4 v40, s[4:7], s47 offen lds\n"
        "buffer_load_dwordx4 v41, s[4:7], s47 offen offset:1024 lds\n"
        "s_waitcnt vmcnt(12)\n"
        "s_barrier\n"
        "s_add_u32 s93, s49, 147456\n"
        "s_lshl4_add_u32 m0, s52, 0\n"
        "s_nop 0\n"
        "buffer_load_dwordx4 v46, s[4:7], s93 offen lds\n"
        "s_add_u32 s49, s49, 128\n"
        "s_cmp_eq_u32 s49, 1152\n"
        "s_cselect_b32 s49, 0, s49\n"
        "s_add_u32 s93, s49, 147456\n"
        "s_lshl4_add_u32 m0, s52, 8192\n"
        "s_nop 0\n"
        "buffer_load_dwordx4 v46, s[4:7], s93 offen lds\n"
        "s_add_u32 s49, s49, 128\n"
        "s_cmp_eq_u32 s49, 1152\n"
        "s_cselect_b32 s49, 0, s49\n"
        "s_add_u32 s93, s49, 147456\n"
        "s_lshl4_add_u32 m0, s52, 16384\n"
        "s_nop 0\n"
        "buffer_load_dwordx4 v46, s[4:7], s93 offen lds\n"
        "s_add_u32 s49, s49, 128\n"
        "s_cmp_eq_u32 s49, 1152\n"
        "s_cselect_b32 s49, 0, s49\n"
        "s_add_u32 s93, s49, 147456\n"
        "s_lshl4_add_u32 m0, s52, 24576\n"
        "s_nop 0\n"
        "buffer_load_dwordx4 v46, s[4:7], s93 offen lds\n"
        "s_add_u32 s49, s49, 128\n"
        "s_cmp_eq_u32 s49, 1152\n"
        "s_cselect_b32 s49, 0, s49\n"
        "s_add_u32 s93, s49, 147456\n"
        "s_lshl4_add_u32 m0, s52, 32768\n"
        "s_nop 0\n"
        "buffer_load_dwordx4 v46, s[4:7], s93 offen lds\n"
        "s_add_u32 s49, s49, 128\n"
        "s_cmp_eq_u32 s49, 1152\n"
        "s_cselect_b32 s49, 0, s49\n"
        "s_add_u32 s93, s49, 147456\n"
        "s_lshl4_add_u32 m0, s52, 40960\n"
        "s_nop 0\n"
        "buffer_load_dwordx4 v46, s[4:7], s93 offen lds\n"
        "s_add_u32 s49, s49, 128\n"
        "s_cmp_eq_u32 s49, 1152\n"
        "s_cselect_b32 s49, 0, s49\n"
        "s_waitcnt vmcnt(12)\n"
        "s_barrier\n"
        "s_add_u32 s93, s49, 147456\n"
        "s_lshl4_add_u32 m0, s52, 49152\n"
        "s_nop 0\n"
        "buffer_load_dwordx4 v46, s[4:7], s93 offen lds\n"
        "s_add_u32 s49, s49, 128\n"
        "s_cmp_eq_u32 s49, 1152\n"
        "s_cselect_b32 s49, 0, s49\n"
        "s_add_u32 s93, s49, 147456\n"
        "s_lshl4_add_u32 m0, s52, 90112\n"
        "s_nop 0\n"
        "buffer_load_dwordx4 v46, s[4:7], s93 offen lds\n"
        "s_add_u32 s49, s49, 128\n"
        "s_cmp_eq_u32 s49, 1152\n"
        "s_cselect_b32 s49, 0, s49\n"
        "s_add_u32 s93, s49, 147456\n"
        "s_lshl4_add_u32 m0, s52, 98304\n"
        "s_nop 0\n"
        "buffer_load_dwordx4 v46, s[4:7], s93 offen lds\n"
        "s_add_u32 s49, s49, 128\n"
        "s_cmp_eq_u32 s49, 1152\n"
        "s_cselect_b32 s49, 0, s49\n"
        "s_add_u32 s93, s49, 184320\n"
        "s_lshl4_add_u32 m0, s52, 4096\n"
        "s_nop 0\n"
        "buffer_load_dwordx4 v46, s[4:7], s93 offen lds\n"
        "s_add_u32 s49, s49, 128\n"
        "s_cmp_eq_u32 s49, 1152\n"
        "s_cselect_b32 s49, 0, s49\n"
        "s_add_u32 s93, s49, 184320\n"
        "s_lshl4_add_u32 m0, s52, 12288\n"
        "s_nop 0\n"
        "buffer_load_dwordx4 v46, s[4:7], s93 offen lds\n"
        "s_add_u32 s49, s49, 128\n"
        "s_cmp_eq_u32 s49, 1152\n"
        "s_cselect_b32 s49, 0, s49\n"
        "s_add_u32 s93, s49, 184320\n"
        "s_lshl4_add_u32 m0, s52, 20480\n"
        "s_nop 0\n"
        "buffer_load_dwordx4 v46, s[4:7], s93 offen lds\n"
        "s_add_u32 s49, s49, 128\n"
        "s_cmp_eq_u32 s49, 1152\n"
        "s_cselect_b32 s49, 0, s49\n"
        "s_waitcnt vmcnt(6)\n"
        "s_barrier\n"
        "s_add_u32 s93, s49, 184320\n"
        "s_lshl4_add_u32 m0, s52, 28672\n"
        "s_nop 0\n"
        "buffer_load_dwordx4 v46, s[4:7], s93 offen lds\n"
        "s_add_u32 s49, s49, 128\n"
        "s_cmp_eq_u32 s49, 1152\n"
        "s_cselect_b32 s49, 0, s49\n"
        "s_add_u32 s93, s49, 184320\n"
        "s_lshl4_add_u32 m0, s52, 36864\n"
        "s_nop 0\n"
        "buffer_load_dwordx4 v46, s[4:7], s93 offen lds\n"
        "s_add_u32 s49, s49, 128\n"
        "s_cmp_eq_u32 s49, 1152\n"
        "s_cselect_b32 s49, 0, s49\n"
        "s_add_u32 s93, s49, 184320\n"
        "s_lshl4_add_u32 m0, s52, 45056\n"
        "s_nop 0\n"
        "buffer_load_dwordx4 v46, s[4:7], s93 offen lds\n"
        "s_add_u32 s49, s49, 128\n"
        "s_cmp_eq_u32 s49, 1152\n"
        "s_cselect_b32 s49, 0, s49\n"
        "s_add_u32 s93, s49, 184320\n"
        "s_lshl4_add_u32 m0, s52, 53248\n"
        "s_nop 0\n"
        "buffer_load_dwordx4 v46, s[4:7], s93 offen lds\n"
        "s_add_u32 s49, s49, 128\n"
        "s_cmp_eq_u32 s49, 1152\n"
        "s_cselect_b32 s49, 0, s49\n"
        "s_add_u32 s93, s49, 184320\n"
        "s_lshl4_add_u32 m0, s52, 94208\n"
        "s_nop 0\n"
        "buffer_load_dwordx4 v46, s[4:7], s93 offen lds\n"
        "s_add_u32 s49, s49, 128\n"
        "s_cmp_eq_u32 s49, 1152\n"
        "s_cselect_b32 s49, 0, s49\n"
        "s_add_u32 s93, s49, 184320\n"
        "s_lshl4_add_u32 m0, s52, 102400\n"
        "s_nop 0\n"
        "buffer_load_dwordx4 v46, s[4:7], s93 offen lds\n"
        "v_and_b32 v12, 31, v0\n"
        "v_and_b32 v13, 24, v0\n"
        "v_add_u32 v12, v12, v13\n"
        "s_and_b32 s93, s2, 15\n"
        "s_mul_i32 s93, s93, 4352\n"
        "v_lshlrev_b32 v12, 3, v12\n"
        "s_add_u32 s93, s93, 12320768\n"
        "buffer_load_dwordx2 v[12:13], v12, s[12:15], s93 offen sc1\n"
        "s_waitcnt vmcnt(1)\n"
        "s_barrier\n"
        "s_waitcnt vmcnt(0) lgkmcnt(0)\n"
        "s_branch .Lmk_join\n"
        ".Lmk_comp:\n"
        "v_lshl_add_u32 v249, s42, 6, v0\n"
        "v_subrev_u32 v248, 280, v249\n"
        "v_mov_b32 v250, 0x3f80\n"
        "v_mov_b32 v251, 0\n"
        "v_lshlrev_b32 v252, 3, v249\n"
        "v_ashrrev_i32 v248, 31, v248\n"
        "v_bfi_b32 v250, v248, 0, v250\n"
        "v_add_u32 v252, 155648, v252\n"
        "ds_write_b64 v252, v[250:251]\n"
        "v_and_b32 v254, 0x3f80, v248\n"
        "v_mov_b32 v255, 0\n"
        "v_min_u32 v252, 159480, v252\n"
        "ds_write_b64 v252, v[254:255] offset:4096\n"
        "s_lshl2_add_u32 s95, s99, s45\n"
        "s_lshl_b32 s92, s95, 5\n"
        "s_add_u32 s92, s92, s98\n"
        "s_mul_i32 s92, s92, 128\n"
        "s_add_u32 s56, s92, 12058624\n"
        "s_cmp_lt_u32 s95, 18\n"
        "s_cselect_b32 s52, 1051597459, 1.0\n"
        "s_mov_b32 s53, s52\n"
        "s_cmp_ge_u32 s95, 54\n"
        "s_cselect_b32 s94, 2147483648, 0\n"
        "s_lshl_b32 s95, s95, 17\n"
        "s_add_u32 s95, s95, s94\n"
        "s_lshl_b32 s98, s93, 9\n"
        "s_add_u32 s95, s95, s98\n"
        "s_add_u32 s95, s95, 1048576\n"
        "v_lshlrev_b32 v249, 14, v14\n"
        "v_lshl_add_u32 v249, v15, 4, v249\n"
        "v_add_u32 v9, s95, v249\n"
        "v_lshrrev_b32 v250, 1, v15\n"
        "v_xor_b32 v250, v250, v14\n"
        "v_lshlrev_b32 v250, 4, v250\n"
        "v_lshl_add_u32 v1, v15, 7, v250\n"
        "v_xor_b32 v2, 64, v1\n"
        "v_add_u32 v3, 81920, v1\n"
        "v_add_u32 v4, 81920, v2\n"
        "v_lshrrev_b32 v249, 2, v15\n"
        "v_and_b32 v250, 1, v14\n"
        "v_lshl_or_b32 v250, v250, 2, v249\n"
        "s_lshl_b32 s94, s45, 1\n"
        "v_xor_b32 v250, s94, v250\n"
        "v_lshl_add_u32 v249, v14, 3, v249\n"
        "v_lshlrev_b32 v249, 8, v249\n"
        "v_lshl_add_u32 v249, v250, 5, v249\n"
        "v_and_b32 v250, 3, v0\n"
        "v_lshl_add_u32 v249, v250, 3, v249\n"
        "v_add_u32 v5, 65536, v249\n"
        "v_xor_b32 v6, 32, v5\n"
        "v_add_u32 v7, 73728, v5\n"
        "v_add_u32 v8, 73728, v6\n"
        "s_waitcnt lgkmcnt(0)\n"
        "s_load_dwordx2 s[40:41], s[12:13], 0x0\n"
        "s_barrier\n"
        "s_waitcnt lgkmcnt(0)\n"
        "s_mul_i32 s98, s40, 0x5851f42d\n"
        "s_mul_hi_u32 s99, s40, 0x4c957f2d\n"
        "s_mul_i32 s92, s41, 0x4c957f2d\n"
        "s_add_u32 s98, s98, s99\n"
        "s_add_u32 s98, s98, s92\n"
        "s_mul_i32 s40, s40, 0x4c957f2d\n"
        "s_add_u32 s40, s40, 0xf767814f\n"
        "s_addc_u32 s41, s98, 0x14057b7e\n"
        "s_waitcnt lgkmcnt(0)\n"
        "s_barrier\n"
        "ds_read_b64_tr_b16 v[112:113], v5 offset:0\n"
        "ds_read_b64_tr_b16 v[114:115], v5 offset:1024\n"
        "ds_read_b128 v[16:19], v1 offset:57344\n"
        "ds_read_b128 v[20:23], v1 offset:59392\n"
        "ds_read_b128 v[24:27], v1 offset:61440\n"
        "ds_read_b128 v[28:31], v1 offset:63488\n"
        "ds_read_b64_tr_b16 v[116:117], v6 offset:0\n"
        "ds_read_b64_tr_b16 v[118:119], v6 offset:1024\n"
        "ds_read_b64_tr_b16 v[120:121], v5 offset:8192\n"
        "ds_read_b64_tr_b16 v[122:123], v5 offset:9216\n"
        "ds_read_b128 v[32:35], v2 offset:57344\n"
        "ds_read_b128 v[36:39], v2 offset:59392\n"
        "ds_read_b128 v[40:43], v2 offset:61440\n"
        "ds_read_b128 v[44:47], v2 offset:63488\n"
        "ds_read_b64_tr_b16 v[124:125], v6 offset:8192\n"
        "ds_read_b64_tr_b16 v[126:127], v6 offset:9216\n"
        "s_waitcnt lgkmcnt(0)\n"
        "s_barrier\n"
        "v_mfma_f32_16x16x32_bf16 v[64:67], v[112:115], v[16:19], 0\n"
        "ds_read_b64_tr_b16 v[128:129], v5 offset:24576\n"
        "v_mfma_f32_16x16x32_bf16 v[68:71], v[112:115], v[20:23], 0\n"
        "ds_read_b64_tr_b16 v[130:131], v5 offset:25600\n"
        "v_mfma_f32_16x16x32_bf16 v[72:75], v[112:115], v[24:27], 0\n"
        "ds_read_b128 v[48:51], v3 offset:0\n"
        "v_mfma_f32_16x16x32_bf16 v[76:79], v[112:115], v[28:31], 0\n"
        "ds_read_b128 v[52:55], v3 offset:2048\n"
        "v_mfma_f32_16x16x32_bf16 v[80:83], v[116:119], v[16:19], 0\n"
        "ds_read_b128 v[56:59], v3 offset:4096\n"
        "v_mfma_f32_16x16x32_bf16 v[84:87], v[116:119], v[20:23], 0\n"
        "ds_read_b128 v[60:63], v3 offset:6144\n"
        "v_mfma_f32_16x16x32_bf16 v[88:91], v[116:119], v[24:27], 0\n"
        "ds_read_b64_tr_b16 v[132:133], v6 offset:24576\n"
        "v_mfma_f32_16x16x32_bf16 v[92:95], v[116:119], v[28:31], 0\n"
        "ds_read_b64_tr_b16 v[134:135], v6 offset:25600\n"
        "v_mfma_f32_16x16x32_bf16 v[64:67], v[120:123], v[32:35], v[64:67]\n"
        "ds_read_b64_tr_b16 v[136:137], v5 offset:32768\n"
        "v_mfma_f32_16x16x32_bf16 v[68:71], v[120:123], v[36:39], v[68:71]\n"
        "ds_read_b64_tr_b16 v[138:139], v5 offset:33792\n"
        "v_mfma_f32_16x16x32_bf16 v[72:75], v[120:123], v[40:43], v[72:75]\n"
        "ds_read_b128 v[16:19], v4 offset:0\n"
        "v_mfma_f32_16x16x32_bf16 v[76:79], v[120:123], v[44:47], v[76:79]\n"
        "ds_read_b128 v[20:23], v4 offset:2048\n"
        "v_mfma_f32_16x16x32_bf16 v[80:83], v[124:127], v[32:35], v[80:83]\n"
        "ds_read_b128 v[24:27], v4 offset:4096\n"
        "v_mfma_f32_16x16x32_bf16 v[84:87], v[124:127], v[36:39], v[84:87]\n"
        "ds_read_b128 v[28:31], v4 offset:6144\n"
        "v_mfma_f32_16x16x32_bf16 v[88:91], v[124:127], v[40:43], v[88:91]\n"
        "ds_read_b64_tr_b16 v[140:141], v6 offset:32768\n"
        "v_mfma_f32_16x16x32_bf16 v[92:95], v[124:127], v[44:47], v[92:95]\n"
        "ds_read_b64_tr_b16 v[142:143], v6 offset:33792\n"
        "s_waitcnt lgkmcnt(0)\n"
        "s_barrier\n"
        "v_mfma_f32_16x16x32_bf16 v[64:67], v[128:131], v[48:51], v[64:67]\n"
        "ds_read_b64_tr_b16 v[144:145], v5 offset:49152\n"
        "v_mfma_f32_16x16x32_bf16 v[68:71], v[128:131], v[52:55], v[68:71]\n"
        "ds_read_b64_tr_b16 v[146:147], v5 offset:50176\n"
        "v_mfma_f32_16x16x32_bf16 v[72:75], v[128:131], v[56:59], v[72:75]\n"
        "ds_read_b128 v[32:35], v3 offset:24576\n"
        "v_mfma_f32_16x16x32_bf16 v[76:79], v[128:131], v[60:63], v[76:79]\n"
        "ds_read_b128 v[36:39], v3 offset:26624\n"
        "v_mfma_f32_16x16x32_bf16 v[80:83], v[132:135], v[48:51], v[80:83]\n"
        "ds_read_b128 v[40:43], v3 offset:28672\n"
        "v_mfma_f32_16x16x32_bf16 v[84:87], v[132:135], v[52:55], v[84:87]\n"
        "ds_read_b128 v[44:47], v3 offset:30720\n"
        "v_mfma_f32_16x16x32_bf16 v[88:91], v[132:135], v[56:59], v[88:91]\n"
        "ds_read_b64_tr_b16 v[148:149], v6 offset:49152\n"
        "v_mfma_f32_16x16x32_bf16 v[92:95], v[132:135], v[60:63], v[92:95]\n"
        "ds_read_b64_tr_b16 v[150:151], v6 offset:50176\n"
        "v_mfma_f32_16x16x32_bf16 v[64:67], v[136:139], v[16:19], v[64:67]\n"
        "ds_read_b64_tr_b16 v[152:153], v5 offset:57344\n"
        "v_mfma_f32_16x16x32_bf16 v[68:71], v[136:139], v[20:23], v[68:71]\n"
        "ds_read_b64_tr_b16 v[154:155], v5 offset:58368\n"
        "v_mfma_f32_16x16x32_bf16 v[72:75], v[136:139], v[24:27], v[72:75]\n"
        "ds_read_b128 v[48:51], v4 offset:24576\n"
        "v_mfma_f32_16x16x32_bf16 v[76:79], v[136:139], v[28:31], v[76:79]\n"
        "ds_read_b128 v[52:55], v4 offset:26624\n"
        "v_mfma_f32_16x16x32_bf16 v[80:83], v[140:143], v[16:19], v[80:83]\n"
        "ds_read_b128 v[56:59], v4 offset:28672\n"
        "v_mfma_f32_16x16x32_bf16 v[84:87], v[140:143], v[20:23], v[84:87]\n"
        "ds_read_b128 v[60:63], v4 offset:30720\n"
        "v_mfma_f32_16x16x32_bf16 v[88:91], v[140:143], v[24:27], v[88:91]\n"
        "ds_read_b64_tr_b16 v[156:157], v6 offset:57344\n"
        "v_mfma_f32_16x16x32_bf16 v[92:95], v[140:143], v[28:31], v[92:95]\n"
        "ds_read_b64_tr_b16 v[158:159], v6 offset:58368\n"
        "s_waitcnt lgkmcnt(0)\n"
        "s_barrier\n"
        "v_mfma_f32_16x16x32_bf16 v[64:67], v[144:147], v[32:35], v[64:67]\n"
        "ds_read_b64_tr_b16 v[160:161], v7 offset:0\n"
        "v_mfma_f32_16x16x32_bf16 v[68:71], v[144:147], v[36:39], v[68:71]\n"
        "ds_read_b64_tr_b16 v[162:163], v7 offset:1024\n"
        "v_mfma_f32_16x16x32_bf16 v[72:75], v[144:147], v[40:43], v[72:75]\n"
        "ds_read_b128 v[16:19], v3 offset:49152\n"
        "v_mfma_f32_16x16x32_bf16 v[76:79], v[144:147], v[44:47], v[76:79]\n"
        "ds_read_b128 v[20:23], v3 offset:51200\n"
        "v_mfma_f32_16x16x32_bf16 v[80:83], v[148:151], v[32:35], v[80:83]\n"
        "ds_read_b128 v[24:27], v3 offset:53248\n"
        "v_mfma_f32_16x16x32_bf16 v[84:87], v[148:151], v[36:39], v[84:87]\n"
        "ds_read_b128 v[28:31], v3 offset:55296\n"
        "v_mfma_f32_16x16x32_bf16 v[88:91], v[148:151], v[40:43], v[88:91]\n"
        "ds_read_b64_tr_b16 v[164:165], v8 offset:0\n"
        "v_mfma_f32_16x16x32_bf16 v[92:95], v[148:151], v[44:47], v[92:95]\n"
        "ds_read_b64_tr_b16 v[166:167], v8 offset:1024\n"
        "v_mfma_f32_16x16x32_bf16 v[64:67], v[152:155], v[48:51], v[64:67]\n"
        "ds_read_b64_tr_b16 v[168:169], v7 offset:8192\n"
        "v_mfma_f32_16x16x32_bf16 v[68:71], v[152:155], v[52:55], v[68:71]\n"
        "ds_read_b64_tr_b16 v[170:171], v7 offset:9216\n"
        "v_mfma_f32_16x16x32_bf16 v[72:75], v[152:155], v[56:59], v[72:75]\n"
        "ds_read_b128 v[32:35], v4 offset:49152\n"
        "v_mfma_f32_16x16x32_bf16 v[76:79], v[152:155], v[60:63], v[76:79]\n"
        "ds_read_b128 v[36:39], v4 offset:51200\n"
        "v_mfma_f32_16x16x32_bf16 v[80:83], v[156:159], v[48:51], v[80:83]\n"
        "ds_read_b128 v[40:43], v4 offset:53248\n"
        "v_mfma_f32_16x16x32_bf16 v[84:87], v[156:159], v[52:55], v[84:87]\n"
        "ds_read_b128 v[44:47], v4 offset:55296\n"
        "v_mfma_f32_16x16x32_bf16 v[88:91], v[156:159], v[56:59], v[88:91]\n"
        "ds_read_b64_tr_b16 v[172:173], v8 offset:8192\n"
        "v_mfma_f32_16x16x32_bf16 v[92:95], v[156:159], v[60:63], v[92:95]\n"
        "ds_read_b64_tr_b16 v[174:175], v8 offset:9216\n"
        "s_waitcnt lgkmcnt(0)\n"
        "s_barrier\n"
        "v_mfma_f32_16x16x32_bf16 v[64:67], v[160:163], v[16:19], v[64:67]\n"
        "ds_read_b64_tr_b16 v[176:177], v5 offset:0\n"
        "v_mfma_f32_16x16x32_bf16 v[68:71], v[160:163], v[20:23], v[68:71]\n"
        "ds_read_b64_tr_b16 v[178:179], v5 offset:1024\n"
        "v_mfma_f32_16x16x32_bf16 v[72:75], v[160:163], v[24:27], v[72:75]\n"
        "ds_read_b128 v[48:51], v1 offset:57344\n"
        "v_mfma_f32_16x16x32_bf16 v[76:79], v[160:163], v[28:31], v[76:79]\n"
        "ds_read_b128 v[52:55], v1 offset:59392\n"
        "v_mfma_f32_16x16x32_bf16 v[80:83], v[164:167], v[16:19], v[80:83]\n"
        "ds_read_b128 v[56:59], v1 offset:61440\n"
        "v_mfma_f32_16x16x32_bf16 v[84:87], v[164:167], v[20:23], v[84:87]\n"
        "ds_read_b128 v[60:63], v1 offset:63488\n"
        "v_mfma_f32_16x16x32_bf16 v[88:91], v[164:167], v[24:27], v[88:91]\n"
        "ds_read_b64_tr_b16 v[180:181], v6 offset:0\n"
        "v_mfma_f32_16x16x32_bf16 v[92:95], v[164:167], v[28:31], v[92:95]\n"
        "ds_read_b64_tr_b16 v[182:183], v6 offset:1024\n"
        "v_mfma_f32_16x16x32_bf16 v[64:67], v[168:171], v[32:35], v[64:67]\n"
        "ds_read_b64_tr_b16 v[184:185], v5 offset:8192\n"
        "v_mfma_f32_16x16x32_bf16 v[68:71], v[168:171], v[36:39], v[68:71]\n"
        "ds_read_b64_tr_b16 v[186:187], v5 offset:9216\n"
        "v_mfma_f32_16x16x32_bf16 v[72:75], v[168:171], v[40:43], v[72:75]\n"
        "ds_read_b128 v[16:19], v2 offset:57344\n"
        "v_mfma_f32_16x16x32_bf16 v[76:79], v[168:171], v[44:47], v[76:79]\n"
        "ds_read_b128 v[20:23], v2 offset:59392\n"
        "v_mfma_f32_16x16x32_bf16 v[80:83], v[172:175], v[32:35], v[80:83]\n"
        "ds_read_b128 v[24:27], v2 offset:61440\n"
        "v_mfma_f32_16x16x32_bf16 v[84:87], v[172:175], v[36:39], v[84:87]\n"
        "ds_read_b128 v[28:31], v2 offset:63488\n"
        "v_mfma_f32_16x16x32_bf16 v[88:91], v[172:175], v[40:43], v[88:91]\n"
        "ds_read_b64_tr_b16 v[188:189], v6 offset:8192\n"
        "v_mfma_f32_16x16x32_bf16 v[92:95], v[172:175], v[44:47], v[92:95]\n"
        "ds_read_b64_tr_b16 v[190:191], v6 offset:9216\n"
        "s_waitcnt lgkmcnt(0)\n"
        "s_barrier\n"
        "v_mfma_f32_16x16x32_bf16 v[64:67], v[176:179], v[48:51], v[64:67]\n"
        "ds_read_b64_tr_b16 v[192:193], v5 offset:24576\n"
        "v_mfma_f32_16x16x32_bf16 v[68:71], v[176:179], v[52:55], v[68:71]\n"
        "ds_read_b64_tr_b16 v[194:195], v5 offset:25600\n"
        "v_mfma_f32_16x16x32_bf16 v[72:75], v[176:179], v[56:59], v[72:75]\n"
        "ds_read_b128 v[32:35], v3 offset:0\n"
        "v_mfma_f32_16x16x32_bf16 v[76:79], v[176:179], v[60:63], v[76:79]\n"
        "ds_read_b128 v[36:39], v3 offset:2048\n"
        "v_mfma_f32_16x16x32_bf16 v[80:83], v[180:183], v[48:51], v[80:83]\n"
        "ds_read_b128 v[40:43], v3 offset:4096\n"
        "v_mfma_f32_16x16x32_bf16 v[84:87], v[180:183], v[52:55], v[84:87]\n"
        "ds_read_b128 v[44:47], v3 offset:6144\n"
        "v_mfma_f32_16x16x32_bf16 v[88:91], v[180:183], v[56:59], v[88:91]\n"
        "ds_read_b64_tr_b16 v[196:197], v6 offset:24576\n"
        "v_mfma_f32_16x16x32_bf16 v[92:95], v[180:183], v[60:63], v[92:95]\n"
        "ds_read_b64_tr_b16 v[198:199], v6 offset:25600\n"
        "v_mfma_f32_16x16x32_bf16 v[64:67], v[184:187], v[16:19], v[64:67]\n"
        "ds_read_b64_tr_b16 v[200:201], v5 offset:32768\n"
        "v_mfma_f32_16x16x32_bf16 v[68:71], v[184:187], v[20:23], v[68:71]\n"
        "ds_read_b64_tr_b16 v[202:203], v5 offset:33792\n"
        "v_mfma_f32_16x16x32_bf16 v[72:75], v[184:187], v[24:27], v[72:75]\n"
        "ds_read_b128 v[48:51], v4 offset:0\n"
        "v_mfma_f32_16x16x32_bf16 v[76:79], v[184:187], v[28:31], v[76:79]\n"
        "ds_read_b128 v[52:55], v4 offset:2048\n"
        "v_mfma_f32_16x16x32_bf16 v[80:83], v[188:191], v[16:19], v[80:83]\n"
        "ds_read_b128 v[56:59], v4 offset:4096\n"
        "v_mfma_f32_16x16x32_bf16 v[84:87], v[188:191], v[20:23], v[84:87]\n"
        "ds_read_b128 v[60:63], v4 offset:6144\n"
        "v_mfma_f32_16x16x32_bf16 v[88:91], v[188:191], v[24:27], v[88:91]\n"
        "ds_read_b64_tr_b16 v[204:205], v6 offset:32768\n"
        "v_mfma_f32_16x16x32_bf16 v[92:95], v[188:191], v[28:31], v[92:95]\n"
        "ds_read_b64_tr_b16 v[206:207], v6 offset:33792\n"
        "s_waitcnt lgkmcnt(0)\n"
        "s_barrier\n"
        "v_mfma_f32_16x16x32_bf16 v[64:67], v[192:195], v[32:35], v[64:67]\n"
        "ds_read_b64_tr_b16 v[208:209], v5 offset:49152\n"
        "v_mfma_f32_16x16x32_bf16 v[68:71], v[192:195], v[36:39], v[68:71]\n"
        "ds_read_b64_tr_b16 v[210:211], v5 offset:50176\n"
        "v_mfma_f32_16x16x32_bf16 v[72:75], v[192:195], v[40:43], v[72:75]\n"
        "ds_read_b128 v[16:19], v3 offset:24576\n"
        "v_mfma_f32_16x16x32_bf16 v[76:79], v[192:195], v[44:47], v[76:79]\n"
        "ds_read_b128 v[20:23], v3 offset:26624\n"
        "v_mfma_f32_16x16x32_bf16 v[80:83], v[196:199], v[32:35], v[80:83]\n"
        "ds_read_b128 v[24:27], v3 offset:28672\n"
        "v_mfma_f32_16x16x32_bf16 v[84:87], v[196:199], v[36:39], v[84:87]\n"
        "ds_read_b128 v[28:31], v3 offset:30720\n"
        "v_mfma_f32_16x16x32_bf16 v[88:91], v[196:199], v[40:43], v[88:91]\n"
        "ds_read_b64_tr_b16 v[212:213], v6 offset:49152\n"
        "v_mfma_f32_16x16x32_bf16 v[92:95], v[196:199], v[44:47], v[92:95]\n"
        "ds_read_b64_tr_b16 v[214:215], v6 offset:50176\n"
        "v_mfma_f32_16x16x32_bf16 v[64:67], v[200:203], v[48:51], v[64:67]\n"
        "ds_read_b64_tr_b16 v[216:217], v5 offset:57344\n"
        "v_mfma_f32_16x16x32_bf16 v[68:71], v[200:203], v[52:55], v[68:71]\n"
        "ds_read_b64_tr_b16 v[218:219], v5 offset:58368\n"
        "v_mfma_f32_16x16x32_bf16 v[72:75], v[200:203], v[56:59], v[72:75]\n"
        "ds_read_b128 v[32:35], v4 offset:24576\n"
        "v_mfma_f32_16x16x32_bf16 v[76:79], v[200:203], v[60:63], v[76:79]\n"
        "ds_read_b128 v[36:39], v4 offset:26624\n"
        "v_mfma_f32_16x16x32_bf16 v[80:83], v[204:207], v[48:51], v[80:83]\n"
        "ds_read_b128 v[40:43], v4 offset:28672\n"
        "v_mfma_f32_16x16x32_bf16 v[84:87], v[204:207], v[52:55], v[84:87]\n"
        "ds_read_b128 v[44:47], v4 offset:30720\n"
        "v_mfma_f32_16x16x32_bf16 v[88:91], v[204:207], v[56:59], v[88:91]\n"
        "ds_read_b64_tr_b16 v[220:221], v6 offset:57344\n"
        "v_mfma_f32_16x16x32_bf16 v[92:95], v[204:207], v[60:63], v[92:95]\n"
        "ds_read_b64_tr_b16 v[222:223], v6 offset:58368\n"
        "s_waitcnt lgkmcnt(0)\n"
        "s_barrier\n"
        "v_mfma_f32_16x16x32_bf16 v[64:67], v[208:211], v[16:19], v[64:67]\n"
        "ds_read_b64_tr_b16 v[224:225], v7 offset:0\n"
        "v_mfma_f32_16x16x32_bf16 v[68:71], v[208:211], v[20:23], v[68:71]\n"
        "ds_read_b64_tr_b16 v[226:227], v7 offset:1024\n"
        "v_mfma_f32_16x16x32_bf16 v[72:75], v[208:211], v[24:27], v[72:75]\n"
        "ds_read_b128 v[48:51], v3 offset:49152\n"
        "v_mfma_f32_16x16x32_bf16 v[76:79], v[208:211], v[28:31], v[76:79]\n"
        "ds_read_b128 v[52:55], v3 offset:51200\n"
        "v_mfma_f32_16x16x32_bf16 v[80:83], v[212:215], v[16:19], v[80:83]\n"
        "ds_read_b128 v[56:59], v3 offset:53248\n"
        "v_mfma_f32_16x16x32_bf16 v[84:87], v[212:215], v[20:23], v[84:87]\n"
        "ds_read_b128 v[60:63], v3 offset:55296\n"
        "v_mfma_f32_16x16x32_bf16 v[88:91], v[212:215], v[24:27], v[88:91]\n"
        "ds_read_b64_tr_b16 v[228:229], v8 offset:0\n"
        "v_mfma_f32_16x16x32_bf16 v[92:95], v[212:215], v[28:31], v[92:95]\n"
        "ds_read_b64_tr_b16 v[230:231], v8 offset:1024\n"
        "v_mfma_f32_16x16x32_bf16 v[64:67], v[216:219], v[32:35], v[64:67]\n"
        "ds_read_b64_tr_b16 v[232:233], v7 offset:8192\n"
        "v_mfma_f32_16x16x32_bf16 v[68:71], v[216:219], v[36:39], v[68:71]\n"
        "ds_read_b64_tr_b16 v[234:235], v7 offset:9216\n"
        "v_mfma_f32_16x16x32_bf16 v[72:75], v[216:219], v[40:43], v[72:75]\n"
        "ds_read_b128 v[16:19], v4 offset:49152\n"
        "v_mfma_f32_16x16x32_bf16 v[76:79], v[216:219], v[44:47], v[76:79]\n"
        "ds_read_b128 v[20:23], v4 offset:51200\n"
        "v_mfma_f32_16x16x32_bf16 v[80:83], v[220:223], v[32:35], v[80:83]\n"
        "ds_read_b128 v[24:27], v4 offset:53248\n"
        "v_mfma_f32_16x16x32_bf16 v[84:87], v[220:223], v[36:39], v[84:87]\n"
        "ds_read_b128 v[28:31], v4 offset:55296\n"
        "v_mfma_f32_16x16x32_bf16 v[88:91], v[220:223], v[40:43], v[88:91]\n"
        "ds_read_b64_tr_b16 v[236:237], v8 offset:8192\n"
        "v_mfma_f32_16x16x32_bf16 v[92:95], v[220:223], v[44:47], v[92:95]\n"
        "ds_read_b64_tr_b16 v[238:239], v8 offset:9216\n"
        "s_waitcnt lgkmcnt(0)\n"
        "s_barrier\n"
        "v_mfma_f32_16x16x32_bf16 v[64:67], v[224:227], v[48:51], v[64:67]\n"
        "ds_read_b64_tr_b16 v[240:241], v5 offset:0\n"
        "v_mfma_f32_16x16x32_bf16 v[68:71], v[224:227], v[52:55], v[68:71]\n"
        "ds_read_b64_tr_b16 v[242:243], v5 offset:1024\n"
        "v_mfma_f32_16x16x32_bf16 v[72:75], v[224:227], v[56:59], v[72:75]\n"
        "ds_read_b128 v[32:35], v1 offset:57344\n"
        "v_mfma_f32_16x16x32_bf16 v[76:79], v[224:227], v[60:63], v[76:79]\n"
        "ds_read_b128 v[36:39], v1 offset:59392\n"
        "v_mfma_f32_16x16x32_bf16 v[80:83], v[228:231], v[48:51], v[80:83]\n"
        "ds_read_b128 v[40:43], v1 offset:61440\n"
        "v_mfma_f32_16x16x32_bf16 v[84:87], v[228:231], v[52:55], v[84:87]\n"
        "ds_read_b128 v[44:47], v1 offset:63488\n"
        "v_mfma_f32_16x16x32_bf16 v[88:91], v[228:231], v[56:59], v[88:91]\n"
        "ds_read_b64_tr_b16 v[244:245], v6 offset:0\n"
        "v_mfma_f32_16x16x32_bf16 v[92:95], v[228:231], v[60:63], v[92:95]\n"
        "ds_read_b64_tr_b16 v[246:247], v6 offset:1024\n"
        "v_mfma_f32_16x16x32_bf16 v[64:67], v[232:235], v[16:19], v[64:67]\n"
        "ds_read_b64_tr_b16 v[248:249], v5 offset:8192\n"
        "v_mfma_f32_16x16x32_bf16 v[68:71], v[232:235], v[20:23], v[68:71]\n"
        "ds_read_b64_tr_b16 v[250:251], v5 offset:9216\n"
        "v_mfma_f32_16x16x32_bf16 v[72:75], v[232:235], v[24:27], v[72:75]\n"
        "ds_read_b128 v[48:51], v2 offset:57344\n"
        "v_mfma_f32_16x16x32_bf16 v[76:79], v[232:235], v[28:31], v[76:79]\n"
        "ds_read_b128 v[52:55], v2 offset:59392\n"
        "v_mfma_f32_16x16x32_bf16 v[80:83], v[236:239], v[16:19], v[80:83]\n"
        "ds_read_b128 v[56:59], v2 offset:61440\n"
        "v_mfma_f32_16x16x32_bf16 v[84:87], v[236:239], v[20:23], v[84:87]\n"
        "ds_read_b128 v[60:63], v2 offset:63488\n"
        "v_mfma_f32_16x16x32_bf16 v[88:91], v[236:239], v[24:27], v[88:91]\n"
        "ds_read_b64_tr_b16 v[252:253], v6 offset:8192\n"
        "v_mfma_f32_16x16x32_bf16 v[92:95], v[236:239], v[28:31], v[92:95]\n"
        "ds_read_b64_tr_b16 v[254:255], v6 offset:9216\n"
        "s_waitcnt lgkmcnt(8)\n"
        "v_mfma_f32_16x16x32_bf16 v[64:67], v[240:243], v[32:35], v[64:67]\n"
        "ds_read_b128 v[16:19], v1 offset:0\n"
        "v_mfma_f32_16x16x32_bf16 v[68:71], v[240:243], v[36:39], v[68:71]\n"
        "ds_read_b128 v[20:23], v1 offset:2048\n"
        "v_mfma_f32_16x16x32_bf16 v[72:75], v[240:243], v[40:43], v[72:75]\n"
        "ds_read_b128 v[24:27], v2 offset:0\n"
        "v_mfma_f32_16x16x32_bf16 v[76:79], v[240:243], v[44:47], v[76:79]\n"
        "ds_read_b128 v[28:31], v2 offset:2048\n"
        "v_mfma_f32_16x16x32_bf16 v[80:83], v[244:247], v[32:35], v[80:83]\n"
        "v_mfma_f32_16x16x32_bf16 v[84:87], v[244:247], v[36:39], v[84:87]\n"
        "v_mfma_f32_16x16x32_bf16 v[88:91], v[244:247], v[40:43], v[88:91]\n"
        "v_mfma_f32_16x16x32_bf16 v[92:95], v[244:247], v[44:47], v[92:95]\n"
        "s_waitcnt lgkmcnt(4)\n"
        "v_mfma_f32_16x16x32_bf16 v[64:67], v[248:251], v[48:51], v[64:67]\n"
        "ds_read_b128 v[32:35], v1 offset:8192\n"
        "v_mfma_f32_16x16x32_bf16 v[68:71], v[248:251], v[52:55], v[68:71]\n"
        "ds_read_b128 v[36:39], v1 offset:10240\n"
        "v_mfma_f32_16x16x32_bf16 v[72:75], v[248:251], v[56:59], v[72:75]\n"
        "ds_read_b128 v[40:43], v2 offset:8192\n"
        "v_mfma_f32_16x16x32_bf16 v[76:79], v[248:251], v[60:63], v[76:79]\n"
        "ds_read_b128 v[44:47], v2 offset:10240\n"
        "v_mfma_f32_16x16x32_bf16 v[80:83], v[252:255], v[48:51], v[80:83]\n"
        "v_mfma_f32_16x16x32_bf16 v[84:87], v[252:255], v[52:55], v[84:87]\n"
        "v_mfma_f32_16x16x32_bf16 v[88:91], v[252:255], v[56:59], v[88:91]\n"
        "v_mfma_f32_16x16x32_bf16 v[92:95], v[252:255], v[60:63], v[92:95]\n"
        "s_waitcnt lgkmcnt(4)\n"
        "v_pk_mul_f32 v[64:65], v[64:65], s[52:53]\n"
        "v_pk_mul_f32 v[66:67], v[66:67], s[52:53]\n"
        "v_cvt_pk_bf16_f32 v96, v64, v65\n"
        "v_cvt_pk_bf16_f32 v97, v66, v67\n"
        "v_mfma_f32_16x16x32_bf16 v[64:67], v[112:115], v[16:19], 0\n"
        "ds_read_b128 v[48:51], v1 offset:16384\n"
        "v_pk_mul_f32 v[68:69], v[68:69], s[52:53]\n"
        "v_pk_mul_f32 v[70:71], v[70:71], s[52:53]\n"
        "v_cvt_pk_bf16_f32 v98, v68, v69\n"
        "v_cvt_pk_bf16_f32 v99, v70, v71\n"
        "v_mfma_f32_16x16x32_bf16 v[68:71], v[112:115], v[20:23], 0\n"
        "s_mov_b32 s93, 0\n"
        "buffer_store_dwordx4 v[96:99], v9, s[12:15], s93 offen offset:0 sc1\n"
        "ds_read_b128 v[52:55], v1 offset:18432\n"
        "v_pk_mul_f32 v[80:81], v[80:81], s[52:53]\n"
        "v_pk_mul_f32 v[82:83], v[82:83], s[52:53]\n"
        "v_cvt_pk_bf16_f32 v104, v80, v81\n"
        "v_cvt_pk_bf16_f32 v105, v82, v83\n"
        "v_mfma_f32_16x16x32_bf16 v[80:83], v[116:119], v[16:19], 0\n"
        "ds_read_b128 v[56:59], v2 offset:16384\n"
        "v_pk_mul_f32 v[84:85], v[84:85], s[52:53]\n"
        "v_pk_mul_f32 v[86:87], v[86:87], s[52:53]\n"
        "v_cvt_pk_bf16_f32 v106, v84, v85\n"
        "v_cvt_pk_bf16_f32 v107, v86, v87\n"
        "v_mfma_f32_16x16x32_bf16 v[84:87], v[116:119], v[20:23], 0\n"
        "s_mov_b32 s93, 65536\n"
        "buffer_store_dwordx4 v[104:107], v9, s[12:15], s93 offen offset:0 sc1\n"
        "ds_read_b128 v[60:63], v2 offset:18432\n"
        "v_mfma_f32_16x16x32_bf16 v[64:67], v[120:123], v[24:27], v[64:67]\n"
        "v_mfma_f32_16x16x32_bf16 v[68:71], v[120:123], v[28:31], v[68:71]\n"
        "v_mfma_f32_16x16x32_bf16 v[80:83], v[124:127], v[24:27], v[80:83]\n"
        "v_mfma_f32_16x16x32_bf16 v[84:87], v[124:127], v[28:31], v[84:87]\n"
        "s_waitcnt lgkmcnt(4)\n"
        "v_mfma_f32_16x16x32_bf16 v[64:67], v[128:131], v[32:35], v[64:67]\n"
        "v_pk_mul_f32 v[72:73], v[72:73], s[52:53]\n"
        "v_pk_mul_f32 v[74:75], v[74:75], s[52:53]\n"
        "v_cvt_pk_bf16_f32 v100, v72, v73\n"
        "v_cvt_pk_bf16_f32 v101, v74, v75\n"
        "ds_read_b128 v[16:19], v1 offset:24576\n"
        "v_mfma_f32_16x16x32_bf16 v[68:71], v[128:131], v[36:39], v[68:71]\n"
        "ds_read_b128 v[20:23], v1 offset:26624\n"
        "v_mfma_f32_16x16x32_bf16 v[80:83], v[132:135], v[32:35], v[80:83]\n"
        "v_pk_mul_f32 v[76:77], v[76:77], s[52:53]\n"
        "v_pk_mul_f32 v[78:79], v[78:79], s[52:53]\n"
        "v_cvt_pk_bf16_f32 v102, v76, v77\n"
        "v_cvt_pk_bf16_f32 v103, v78, v79\n"
        "s_mov_b32 s93, 0\n"
        "buffer_store_dwordx4 v[100:103], v9, s[12:15], s93 offen offset:256 sc1\n"
        "ds_read_b128 v[24:27], v2 offset:24576\n"
        "v_mfma_f32_16x16x32_bf16 v[84:87], v[132:135], v[36:39], v[84:87]\n"
        "ds_read_b128 v[28:31], v2 offset:26624\n"
        "v_mfma_f32_16x16x32_bf16 v[64:67], v[136:139], v[40:43], v[64:67]\n"
        "v_pk_mul_f32 v[88:89], v[88:89], s[52:53]\n"
        "v_pk_mul_f32 v[90:91], v[90:91], s[52:53]\n"
        "v_cvt_pk_bf16_f32 v108, v88, v89\n"
        "v_cvt_pk_bf16_f32 v109, v90, v91\n"
        "v_mfma_f32_16x16x32_bf16 v[68:71], v[136:139], v[44:47], v[68:71]\n"
        "v_mfma_f32_16x16x32_bf16 v[80:83], v[140:143], v[40:43], v[80:83]\n"
        "v_pk_mul_f32 v[92:93], v[92:93], s[52:53]\n"
        "v_pk_mul_f32 v[94:95], v[94:95], s[52:53]\n"
        "v_cvt_pk_bf16_f32 v110, v92, v93\n"
        "v_cvt_pk_bf16_f32 v111, v94, v95\n"
        "s_mov_b32 s93, 65536\n"
        "buffer_store_dwordx4 v[108:111], v9, s[12:15], s93 offen offset:256 sc1\n"
        "v_mfma_f32_16x16x32_bf16 v[84:87], v[140:143], v[44:47], v[84:87]\n"
        "s_barrier\n"
        "s_waitcnt lgkmcnt(4)\n"
        "v_mfma_f32_16x16x32_bf16 v[64:67], v[144:147], v[48:51], v[64:67]\n"
        "ds_read_b128 v[32:35], v1 offset:32768\n"
        "v_mfma_f32_16x16x32_bf16 v[68:71], v[144:147], v[52:55], v[68:71]\n"
        "ds_read_b128 v[36:39], v1 offset:34816\n"
        "v_mfma_f32_16x16x32_bf16 v[80:83], v[148:151], v[48:51], v[80:83]\n"
        "ds_read_b128 v[40:43], v2 offset:32768\n"
        "v_mfma_f32_16x16x32_bf16 v[84:87], v[148:151], v[52:55], v[84:87]\n"
        "ds_read_b128 v[44:47], v2 offset:34816\n"
        "v_mfma_f32_16x16x32_bf16 v[64:67], v[152:155], v[56:59], v[64:67]\n"
        "v_mfma_f32_16x16x32_bf16 v[68:71], v[152:155], v[60:63], v[68:71]\n"
        "v_mfma_f32_16x16x32_bf16 v[80:83], v[156:159], v[56:59], v[80:83]\n"
        "v_mfma_f32_16x16x32_bf16 v[84:87], v[156:159], v[60:63], v[84:87]\n"
        "s_waitcnt lgkmcnt(4)\n"
        "v_mfma_f32_16x16x32_bf16 v[64:67], v[160:163], v[16:19], v[64:67]\n"
        "ds_read_b128 v[48:51], v1 offset:40960\n"
        "v_mfma_f32_16x16x32_bf16 v[68:71], v[160:163], v[20:23], v[68:71]\n"
        "ds_read_b128 v[52:55], v1 offset:43008\n"
        "v_mfma_f32_16x16x32_bf16 v[80:83], v[164:167], v[16:19], v[80:83]\n"
        "ds_read_b128 v[56:59], v2 offset:40960\n"
        "v_mfma_f32_16x16x32_bf16 v[84:87], v[164:167], v[20:23], v[84:87]\n"
        "ds_read_b128 v[60:63], v2 offset:43008\n"
        "v_mfma_f32_16x16x32_bf16 v[64:67], v[168:171], v[24:27], v[64:67]\n"
        "v_mfma_f32_16x16x32_bf16 v[68:71], v[168:171], v[28:31], v[68:71]\n"
        "v_mfma_f32_16x16x32_bf16 v[80:83], v[172:175], v[24:27], v[80:83]\n"
        "v_mfma_f32_16x16x32_bf16 v[84:87], v[172:175], v[28:31], v[84:87]\n"
        "s_waitcnt lgkmcnt(4)\n"
        "v_mfma_f32_16x16x32_bf16 v[64:67], v[176:179], v[32:35], v[64:67]\n"
        "ds_read_b128 v[16:19], v1 offset:49152\n"
        "v_mfma_f32_16x16x32_bf16 v[68:71], v[176:179], v[36:39], v[68:71]\n"
        "ds_read_b128 v[20:23], v1 offset:51200\n"
        "v_mfma_f32_16x16x32_bf16 v[80:83], v[180:183], v[32:35], v[80:83]\n"
        "ds_read_b128 v[24:27], v2 offset:49152\n"
        "v_mfma_f32_16x16x32_bf16 v[84:87], v[180:183], v[36:39], v[84:87]\n"
        "ds_read_b128 v[28:31], v2 offset:51200\n"
        "v_mfma_f32_16x16x32_bf16 v[64:67], v[184:187], v[40:43], v[64:67]\n"
        "v_mfma_f32_16x16x32_bf16 v[68:71], v[184:187], v[44:47], v[68:71]\n"
        "v_mfma_f32_16x16x32_bf16 v[80:83], v[188:191], v[40:43], v[80:83]\n"
        "v_mfma_f32_16x16x32_bf16 v[84:87], v[188:191], v[44:47], v[84:87]\n"
        "s_waitcnt lgkmcnt(4)\n"
        "v_mfma_f32_16x16x32_bf16 v[64:67], v[192:195], v[48:51], v[64:67]\n"
        "ds_read_b128 v[32:35], v3 offset:8192\n"
        "v_mfma_f32_16x16x32_bf16 v[68:71], v[192:195], v[52:55], v[68:71]\n"
        "ds_read_b128 v[36:39], v3 offset:10240\n"
        "v_mfma_f32_16x16x32_bf16 v[80:83], v[196:199], v[48:51], v[80:83]\n"
        "ds_read_b128 v[40:43], v4 offset:8192\n"
        "v_mfma_f32_16x16x32_bf16 v[84:87], v[196:199], v[52:55], v[84:87]\n"
        "ds_read_b128 v[44:47], v4 offset:10240\n"
        "v_mfma_f32_16x16x32_bf16 v[64:67], v[200:203], v[56:59], v[64:67]\n"
        "v_mfma_f32_16x16x32_bf16 v[68:71], v[200:203], v[60:63], v[68:71]\n"
        "v_mfma_f32_16x16x32_bf16 v[80:83], v[204:207], v[56:59], v[80:83]\n"
        "v_mfma_f32_16x16x32_bf16 v[84:87], v[204:207], v[60:63], v[84:87]\n"
        "s_waitcnt lgkmcnt(4)\n"
        "v_mfma_f32_16x16x32_bf16 v[64:67], v[208:211], v[16:19], v[64:67]\n"
        "ds_read_b128 v[48:51], v3 offset:16384\n"
        "v_mfma_f32_16x16x32_bf16 v[68:71], v[208:211], v[20:23], v[68:71]\n"
        "ds_read_b128 v[52:55], v3 offset:18432\n"
        "v_mfma_f32_16x16x32_bf16 v[80:83], v[212:215], v[16:19], v[80:83]\n"
        "ds_read_b128 v[56:59], v4 offset:16384\n"
        "v_mfma_f32_16x16x32_bf16 v[84:87], v[212:215], v[20:23], v[84:87]\n"
        "ds_read_b128 v[60:63], v4 offset:18432\n"
        "v_mfma_f32_16x16x32_bf16 v[64:67], v[216:219], v[24:27], v[64:67]\n"
        "v_mfma_f32_16x16x32_bf16 v[68:71], v[216:219], v[28:31], v[68:71]\n"
        "v_mfma_f32_16x16x32_bf16 v[80:83], v[220:223], v[24:27], v[80:83]\n"
        "v_mfma_f32_16x16x32_bf16 v[84:87], v[220:223], v[28:31], v[84:87]\n"
        "s_waitcnt lgkmcnt(4)\n"
        "v_mfma_f32_16x16x32_bf16 v[64:67], v[224:227], v[32:35], v[64:67]\n"
        "ds_read_b128 v[16:19], v1 offset:4096\n"
        "v_mfma_f32_16x16x32_bf16 v[68:71], v[224:227], v[36:39], v[68:71]\n"
        "ds_read_b128 v[20:23], v1 offset:6144\n"
        "v_mfma_f32_16x16x32_bf16 v[80:83], v[228:231], v[32:35], v[80:83]\n"
        "ds_read_b128 v[24:27], v2 offset:4096\n"
        "v_mfma_f32_16x16x32_bf16 v[84:87], v[228:231], v[36:39], v[84:87]\n"
        "ds_read_b128 v[28:31], v2 offset:6144\n"
        "v_mfma_f32_16x16x32_bf16 v[64:67], v[232:235], v[40:43], v[64:67]\n"
        "v_mfma_f32_16x16x32_bf16 v[68:71], v[232:235], v[44:47], v[68:71]\n"
        "v_mfma_f32_16x16x32_bf16 v[80:83], v[236:239], v[40:43], v[80:83]\n"
        "v_mfma_f32_16x16x32_bf16 v[84:87], v[236:239], v[44:47], v[84:87]\n"
        "s_waitcnt lgkmcnt(4)\n"
        "v_mfma_f32_16x16x32_bf16 v[64:67], v[240:243], v[48:51], v[64:67]\n"
        "ds_read_b128 v[32:35], v1 offset:12288\n"
        "v_mfma_f32_16x16x32_bf16 v[68:71], v[240:243], v[52:55], v[68:71]\n"
        "ds_read_b128 v[36:39], v1 offset:14336\n"
        "v_mfma_f32_16x16x32_bf16 v[80:83], v[244:247], v[48:51], v[80:83]\n"
        "ds_read_b128 v[40:43], v2 offset:12288\n"
        "v_mfma_f32_16x16x32_bf16 v[84:87], v[244:247], v[52:55], v[84:87]\n"
        "ds_read_b128 v[44:47], v2 offset:14336\n"
        "v_mfma_f32_16x16x32_bf16 v[64:67], v[248:251], v[56:59], v[64:67]\n"
        "v_mfma_f32_16x16x32_bf16 v[68:71], v[248:251], v[60:63], v[68:71]\n"
        "v_mfma_f32_16x16x32_bf16 v[80:83], v[252:255], v[56:59], v[80:83]\n"
        "v_mfma_f32_16x16x32_bf16 v[84:87], v[252:255], v[60:63], v[84:87]\n"
        "s_waitcnt lgkmcnt(4)\n"
        "v_mfma_f32_16x16x32_bf16 v[72:75], v[112:115], v[16:19], 0\n"
        "s_nop 2\n"
        "v_pk_mul_f32 v[64:65], v[64:65], s[52:53]\n"
        "v_pk_mul_f32 v[66:67], v[66:67], s[52:53]\n"
        "v_cvt_pk_bf16_f32 v96, v64, v65\n"
        "v_cvt_pk_bf16_f32 v97, v66, v67\n"
        "ds_read_b128 v[48:51], v1 offset:20480\n"
        "v_mfma_f32_16x16x32_bf16 v[76:79], v[112:115], v[20:23], 0\n"
        "ds_read_b128 v[52:55], v1 offset:22528\n"
        "v_mfma_f32_16x16x32_bf16 v[88:91], v[116:119], v[16:19], 0\n"
        "v_pk_mul_f32 v[68:69], v[68:69], s[52:53]\n"
        "v_pk_mul_f32 v[70:71], v[70:71], s[52:53]\n"
        "v_cvt_pk_bf16_f32 v98, v68, v69\n"
        "v_cvt_pk_bf16_f32 v99, v70, v71\n"
        "s_mov_b32 s93, 1024\n"
        "buffer_store_dwordx4 v[96:99], v9, s[12:15], s93 offen offset:0 sc1\n"
        "ds_read_b128 v[56:59], v2 offset:20480\n"
        "v_mfma_f32_16x16x32_bf16 v[92:95], v[116:119], v[20:23], 0\n"
        "ds_read_b128 v[60:63], v2 offset:22528\n"
        "v_mfma_f32_16x16x32_bf16 v[72:75], v[120:123], v[24:27], v[72:75]\n"
        "v_pk_mul_f32 v[80:81], v[80:81], s[52:53]\n"
        "v_pk_mul_f32 v[82:83], v[82:83], s[52:53]\n"
        "v_cvt_pk_bf16_f32 v104, v80, v81\n"
        "v_cvt_pk_bf16_f32 v105, v82, v83\n"
        "v_mfma_f32_16x16x32_bf16 v[76:79], v[120:123], v[28:31], v[76:79]\n"
        "v_mfma_f32_16x16x32_bf16 v[88:91], v[124:127], v[24:27], v[88:91]\n"
        "v_pk_mul_f32 v[84:85], v[84:85], s[52:53]\n"
        "v_pk_mul_f32 v[86:87], v[86:87], s[52:53]\n"
        "v_cvt_pk_bf16_f32 v106, v84, v85\n"
        "v_cvt_pk_bf16_f32 v107, v86, v87\n"
        "s_mov_b32 s93, 66560\n"
        "buffer_store_dwordx4 v[104:107], v9, s[12:15], s93 offen offset:0 sc1\n"
        "v_mfma_f32_16x16x32_bf16 v[92:95], v[124:127], v[28:31], v[92:95]\n"
        "s_waitcnt lgkmcnt(4)\n"
        "v_mfma_f32_16x16x32_bf16 v[72:75], v[128:131], v[32:35], v[72:75]\n"
        "ds_read_b128 v[16:19], v1 offset:28672\n"
        "v_mfma_f32_16x16x32_bf16 v[76:79], v[128:131], v[36:39], v[76:79]\n"
        "ds_read_b128 v[20:23], v1 offset:30720\n"
        "v_mfma_f32_16x16x32_bf16 v[88:91], v[132:135], v[32:35], v[88:91]\n"
        "ds_read_b128 v[24:27], v2 offset:28672\n"
        "v_mfma_f32_16x16x32_bf16 v[92:95], v[132:135], v[36:39], v[92:95]\n"
        "ds_read_b128 v[28:31], v2 offset:30720\n"
        "v_mfma_f32_16x16x32_bf16 v[72:75], v[136:139], v[40:43], v[72:75]\n"
        "v_mfma_f32_16x16x32_bf16 v[76:79], v[136:139], v[44:47], v[76:79]\n"
        "v_mfma_f32_16x16x32_bf16 v[88:91], v[140:143], v[40:43], v[88:91]\n"
        "v_mfma_f32_16x16x32_bf16 v[92:95], v[140:143], v[44:47], v[92:95]\n"
        "s_waitcnt vmcnt(2)\n"
        "v_mov_b32 v10, s40\n"
        "v_mov_b32 v11, s41\n"
        "s_mov_b64 exec, 1\n"
        "buffer_store_dwordx2 v[10:11], off, s[12:15], s56 sc1\n"
        "s_mov_b64 exec, -1\n"
        "s_add_u32 s56, s56, 2048\n"
        "s_waitcnt lgkmcnt(4)\n"
        "v_mfma_f32_16x16x32_bf16 v[72:75], v[144:147], v[48:51], v[72:75]\n"
        "ds_read_b128 v[32:35], v1 offset:36864\n"
        "v_mfma_f32_16x16x32_bf16 v[76:79], v[144:147], v[52:55], v[76:79]\n"
        "ds_read_b128 v[36:39], v1 offset:38912\n"
        "v_mfma_f32_16x16x32_bf16 v[88:91], v[148:151], v[48:51], v[88:91]\n"
        "ds_read_b128 v[40:43], v2 offset:36864\n"
        "v_mfma_f32_16x16x32_bf16 v[92:95], v[148:151], v[52:55], v[92:95]\n"
        "ds_read_b128 v[44:47], v2 offset:38912\n"
        "v_mfma_f32_16x16x32_bf16 v[72:75], v[152:155], v[56:59], v[72:75]\n"
        "v_mfma_f32_16x16x32_bf16 v[76:79], v[152:155], v[60:63], v[76:79]\n"
        "v_mfma_f32_16x16x32_bf16 v[88:91], v[156:159], v[56:59], v[88:91]\n"
        "v_mfma_f32_16x16x32_bf16 v[92:95], v[156:159], v[60:63], v[92:95]\n"
        "s_waitcnt lgkmcnt(4)\n"
        "v_mfma_f32_16x16x32_bf16 v[72:75], v[160:163], v[16:19], v[72:75]\n"
        "ds_read_b128 v[48:51], v1 offset:45056\n"
        "v_mfma_f32_16x16x32_bf16 v[76:79], v[160:163], v[20:23], v[76:79]\n"
        "ds_read_b128 v[52:55], v1 offset:47104\n"
        "v_mfma_f32_16x16x32_bf16 v[88:91], v[164:167], v[16:19], v[88:91]\n"
        "ds_read_b128 v[56:59], v2 offset:45056\n"
        "v_mfma_f32_16x16x32_bf16 v[92:95], v[164:167], v[20:23], v[92:95]\n"
        "ds_read_b128 v[60:63], v2 offset:47104\n"
        "v_mfma_f32_16x16x32_bf16 v[72:75], v[168:171], v[24:27], v[72:75]\n"
        "v_mfma_f32_16x16x32_bf16 v[76:79], v[168:171], v[28:31], v[76:79]\n"
        "v_mfma_f32_16x16x32_bf16 v[88:91], v[172:175], v[24:27], v[88:91]\n"
        "v_mfma_f32_16x16x32_bf16 v[92:95], v[172:175], v[28:31], v[92:95]\n"
        "s_waitcnt lgkmcnt(4)\n"
        "v_mfma_f32_16x16x32_bf16 v[72:75], v[176:179], v[32:35], v[72:75]\n"
        "ds_read_b128 v[16:19], v1 offset:53248\n"
        "v_mfma_f32_16x16x32_bf16 v[76:79], v[176:179], v[36:39], v[76:79]\n"
        "ds_read_b128 v[20:23], v1 offset:55296\n"
        "v_mfma_f32_16x16x32_bf16 v[88:91], v[180:183], v[32:35], v[88:91]\n"
        "ds_read_b128 v[24:27], v2 offset:53248\n"
        "v_mfma_f32_16x16x32_bf16 v[92:95], v[180:183], v[36:39], v[92:95]\n"
        "ds_read_b128 v[28:31], v2 offset:55296\n"
        "v_mfma_f32_16x16x32_bf16 v[72:75], v[184:187], v[40:43], v[72:75]\n"
        "v_mfma_f32_16x16x32_bf16 v[76:79], v[184:187], v[44:47], v[76:79]\n"
        "v_mfma_f32_16x16x32_bf16 v[88:91], v[188:191], v[40:43], v[88:91]\n"
        "v_mfma_f32_16x16x32_bf16 v[92:95], v[188:191], v[44:47], v[92:95]\n"
        "s_waitcnt lgkmcnt(4)\n"
        "v_mfma_f32_16x16x32_bf16 v[72:75], v[192:195], v[48:51], v[72:75]\n"
        "ds_read_b128 v[32:35], v3 offset:12288\n"
        "v_mfma_f32_16x16x32_bf16 v[76:79], v[192:195], v[52:55], v[76:79]\n"
        "ds_read_b128 v[36:39], v3 offset:14336\n"
        "v_mfma_f32_16x16x32_bf16 v[88:91], v[196:199], v[48:51], v[88:91]\n"
        "ds_read_b128 v[40:43], v4 offset:12288\n"
        "v_mfma_f32_16x16x32_bf16 v[92:95], v[196:199], v[52:55], v[92:95]\n"
        "ds_read_b128 v[44:47], v4 offset:14336\n"
        "v_mfma_f32_16x16x32_bf16 v[72:75], v[200:203], v[56:59], v[72:75]\n"
        "v_mfma_f32_16x16x32_bf16 v[76:79], v[200:203], v[60:63], v[76:79]\n"
        "v_mfma_f32_16x16x32_bf16 v[88:91], v[204:207], v[56:59], v[88:91]\n"
        "v_mfma_f32_16x16x32_bf16 v[92:95], v[204:207], v[60:63], v[92:95]\n"
        "s_waitcnt lgkmcnt(4)\n"
        "v_mfma_f32_16x16x32_bf16 v[72:75], v[208:211], v[16:19], v[72:75]\n"
        "ds_read_b128 v[48:51], v3 offset:20480\n"
        "v_mfma_f32_16x16x32_bf16 v[76:79], v[208:211], v[20:23], v[76:79]\n"
        "ds_read_b128 v[52:55], v3 offset:22528\n"
        "v_mfma_f32_16x16x32_bf16 v[88:91], v[212:215], v[16:19], v[88:91]\n"
        "ds_read_b128 v[56:59], v4 offset:20480\n"
        "v_mfma_f32_16x16x32_bf16 v[92:95], v[212:215], v[20:23], v[92:95]\n"
        "ds_read_b128 v[60:63], v4 offset:22528\n"
        "v_mfma_f32_16x16x32_bf16 v[72:75], v[216:219], v[24:27], v[72:75]\n"
        "v_mfma_f32_16x16x32_bf16 v[76:79], v[216:219], v[28:31], v[76:79]\n"
        "v_mfma_f32_16x16x32_bf16 v[88:91], v[220:223], v[24:27], v[88:91]\n"
        "v_mfma_f32_16x16x32_bf16 v[92:95], v[220:223], v[28:31], v[92:95]\n"
        "s_waitcnt lgkmcnt(4)\n"
        "v_mfma_f32_16x16x32_bf16 v[72:75], v[224:227], v[32:35], v[72:75]\n"
        "v_mfma_f32_16x16x32_bf16 v[76:79], v[224:227], v[36:39], v[76:79]\n"
        "v_mfma_f32_16x16x32_bf16 v[88:91], v[228:231], v[32:35], v[88:91]\n"
        "v_mfma_f32_16x16x32_bf16 v[92:95], v[228:231], v[36:39], v[92:95]\n"
        "v_mfma_f32_16x16x32_bf16 v[72:75], v[232:235], v[40:43], v[72:75]\n"
        "v_mfma_f32_16x16x32_bf16 v[76:79], v[232:235], v[44:47], v[76:79]\n"
        "v_mfma_f32_16x16x32_bf16 v[88:91], v[236:239], v[40:43], v[88:91]\n"
        "v_mfma_f32_16x16x32_bf16 v[92:95], v[236:239], v[44:47], v[92:95]\n"
        "s_waitcnt lgkmcnt(0)\n"
        "v_mfma_f32_16x16x32_bf16 v[72:75], v[240:243], v[48:51], v[72:75]\n"
        "v_mfma_f32_16x16x32_bf16 v[76:79], v[240:243], v[52:55], v[76:79]\n"
        "v_mfma_f32_16x16x32_bf16 v[88:91], v[244:247], v[48:51], v[88:91]\n"
        "v_mfma_f32_16x16x32_bf16 v[92:95], v[244:247], v[52:55], v[92:95]\n"
        "v_mfma_f32_16x16x32_bf16 v[72:75], v[248:251], v[56:59], v[72:75]\n"
        "v_mfma_f32_16x16x32_bf16 v[76:79], v[248:251], v[60:63], v[76:79]\n"
        "v_mfma_f32_16x16x32_bf16 v[88:91], v[252:255], v[56:59], v[88:91]\n"
        "v_mfma_f32_16x16x32_bf16 v[92:95], v[252:255], v[60:63], v[92:95]\n"
        "s_nop 4\n"
        "v_pk_mul_f32 v[72:73], v[72:73], s[52:53]\n"
        "v_pk_mul_f32 v[74:75], v[74:75], s[52:53]\n"
        "v_cvt_pk_bf16_f32 v100, v72, v73\n"
        "v_cvt_pk_bf16_f32 v101, v74, v75\n"
        "v_pk_mul_f32 v[76:77], v[76:77], s[52:53]\n"
        "v_pk_mul_f32 v[78:79], v[78:79], s[52:53]\n"
        "v_cvt_pk_bf16_f32 v102, v76, v77\n"
        "v_cvt_pk_bf16_f32 v103, v78, v79\n"
        "s_mov_b32 s93, 1024\n"
        "buffer_store_dwordx4 v[100:103], v9, s[12:15], s93 offen offset:256 sc1\n"
        "v_pk_mul_f32 v[88:89], v[88:89], s[52:53]\n"
        "v_pk_mul_f32 v[90:91], v[90:91], s[52:53]\n"
        "v_cvt_pk_bf16_f32 v108, v88, v89\n"
        "v_cvt_pk_bf16_f32 v109, v90, v91\n"
        "v_pk_mul_f32 v[92:93], v[92:93], s[52:53]\n"
        "v_pk_mul_f32 v[94:95], v[94:95], s[52:53]\n"
        "v_cvt_pk_bf16_f32 v110, v92, v93\n"
        "v_cvt_pk_bf16_f32 v111, v94, v95\n"
        "s_mov_b32 s93, 66560\n"
        "buffer_store_dwordx4 v[108:111], v9, s[12:15], s93 offen offset:256 sc1\n"
        ".Lmk_join:\n"
        "s_cmp_eq_u32 s46, 1\n"
        "s_cbranch_scc0 .Lmk_s5\n"
        "v_mov_b32 v254, 155672\n"
        "v_mov_b32 v255, 1\n"
        "s_mov_b64 exec, 1\n"
        "ds_add_u32 v254, v255\n"
        "s_mov_b64 exec, -1\n"
        ".Lmk_s5:\n"
        "s_cmp_eq_u32 s46, 0\n"
        "s_cbranch_scc0 .Lmk_s6\n"
        "v_cmp_ne_u32 vcc, s40, v13\n"
        "s_nop 0\n"
        "s_cmp_eq_u64 vcc, 0\n"
        "s_cbranch_scc0 .Lmk_rgspin\n"
        ".Lmk_rgok:\n"
        "v_max_u32_dpp v12, v12, v12 row_ror:8 row_mask:0xf bank_mask:0xf\n"
        "s_nop 1\n"
        "v_max_u32_dpp v12, v12, v12 row_ror:4 row_mask:0xf bank_mask:0xf\n"
        "s_nop 1\n"
        "v_max_u32_dpp v12, v12, v12 row_ror:2 row_mask:0xf bank_mask:0xf\n"
        "s_nop 1\n"
        "v_max_u32_dpp v12, v12, v12 row_ror:1 row_mask:0xf bank_mask:0xf\n"
        "v_lshlrev_b32 v248, 4, v14\n"
        "v_readlane_b32 s98, v12, 0\n"
        "v_readlane_b32 s99, v12, 16\n"
        "s_nop 0\n"
        "s_max_u32 s58, s98, s99\n"
        "s_andn2_b32 s57, 0x7ff, s58\n"
        "s_mul_i32 s98, s57, 1152\n"
        "s_add_u32 s98, s98, s97\n"
        "buffer_load_dwordx4 v[80:83], v248, s[4:7], s98 offen offset:0\n"
        "buffer_load_dwordx4 v[84:87], v248, s[4:7], s98 offen offset:64\n"
        "buffer_load_dwordx4 v[88:91], v248, s[4:7], s98 offen offset:128\n"
        "buffer_load_dwordx4 v[92:95], v248, s[4:7], s98 offen offset:192\n"
        "buffer_load_dwordx4 v[96:99], v248, s[4:7], s98 offen offset:256\n"
        "buffer_load_dwordx4 v[100:103], v248, s[4:7], s98 offen offset:320\n"
        "s_cmp_lt_u32 s44, 28\n"
        "s_cbranch_scc0 .Lmk_s7\n"
        "s_add_u32 m0, s55, 106496\n"
        "s_nop 0\n"
        "buffer_load_dwordx4 v1, s[16:19], s54 offen lds\n"
        "buffer_load_dwordx4 v2, s[16:19], s54 offen offset:1024 lds\n"
        "s_add_u32 s93, s54, 589824\n"
        "s_add_u32 m0, s55, 114688\n"
        "s_nop 0\n"
        "buffer_load_dwordx4 v1, s[16:19], s93 offen lds\n"
        "buffer_load_dwordx4 v2, s[16:19], s93 offen offset:1024 lds\n"
        "s_add_u32 s93, s54, 1179648\n"
        "s_add_u32 m0, s55, 122880\n"
        "s_nop 0\n"
        "buffer_load_dwordx4 v1, s[16:19], s93 offen lds\n"
        "buffer_load_dwordx4 v2, s[16:19], s93 offen offset:1024 lds\n"
        ".Lmk_s7:\n"
        "v_mov_b32 v250, 155696\n"
        "v_mov_b32 v251, s58\n"
        "ds_write_b32 v250, v251\n"
        ".Lmk_s6:\n"
        "s_and_b32 s98, s44, 3\n"
        "s_lshl2_add_u32 s60, s43, s98\n"
        "s_and_b32 s61, s60, 1\n"
        "s_lshr_b32 s59, s44, 2\n"
        "s_lshl2_add_u32 s62, s46, s45\n"
        "s_lshr_b32 s98, s60, 1\n"
        "s_mul_i32 s63, s98, 9\n"
        "s_mul_i32 s98, s61, 5\n"
        "s_add_u32 s3, s63, s98\n"
        "s_lshl3_add_u32 s100, s59, s62\n"
        "v_lshrrev_b32 v10, 5, v0\n"
        "v_and_b32 v249, 31, v0\n"
        "v_lshrrev_b32 v250, 1, v249\n"
        "v_and_b32 v251, 1, v249\n"
        "v_lshl_add_u32 v250, v251, 4, v250\n"
        "v_lshlrev_b32 v251, 1, v10\n"
        "v_sub_u32 v250, v250, v251\n"
        "v_cvt_f32_i32 v250, v250\n"
        "v_add_f32 v250, 0.5, v250\n"
        "v_mul_f32 v250, 0x71800000, v250\n"
        "v_add_f32 v104, 0, v250\n"
        "v_add_f32 v105, 0xf3800000, v250\n"
        "v_add_f32 v106, 0xf1800000, v250\n"
        "v_add_f32 v107, 0xf3880000, v250\n"
        "v_add_f32 v108, 0xf2800000, v250\n"
        "v_add_f32 v109, 0xf3a00000, v250\n"
        "v_add_f32 v110, 0xf2a00000, v250\n"
        "v_add_f32 v111, 0xf3a80000, v250\n"
        "v_add_f32 v112, 0xf3000000, v250\n"
        "v_add_f32 v113, 0xf3c00000, v250\n"
        "v_add_f32 v114, 0xf3100000, v250\n"
        "v_add_f32 v115, 0xf3c80000, v250\n"
        "v_add_f32 v116, 0xf3400000, v250\n"
        "v_add_f32 v117, 0xf3e00000, v250\n"
        "v_add_f32 v118, 0xf3500000, v250\n"
        "v_add_f32 v119, 0xf3e80000, v250\n"
        "v_min_f32 v120, 0, v104\n"
        "v_min_f32_e64 v104, -v104, 0\n"
        "v_min_f32 v121, 0, v105\n"
        "v_min_f32_e64 v105, -v105, 0\n"
        "v_min_f32 v122, 0, v106\n"
        "v_min_f32_e64 v106, -v106, 0\n"
        "v_min_f32 v123, 0, v107\n"
        "v_min_f32_e64 v107, -v107, 0\n"
        "v_min_f32 v124, 0, v108\n"
        "v_min_f32_e64 v108, -v108, 0\n"
        "v_min_f32 v125, 0, v109\n"
        "v_min_f32_e64 v109, -v109, 0\n"
        "v_min_f32 v126, 0, v110\n"
        "v_min_f32_e64 v110, -v110, 0\n"
        "v_min_f32 v127, 0, v111\n"
        "v_min_f32_e64 v111, -v111, 0\n"
        "v_min_f32 v128, 0, v112\n"
        "v_min_f32_e64 v112, -v112, 0\n"
        "v_min_f32 v129, 0, v113\n"
        "v_min_f32_e64 v113, -v113, 0\n"
        "v_min_f32 v130, 0, v114\n"
        "v_min_f32_e64 v114, -v114, 0\n"
        "v_min_f32 v131, 0, v115\n"
        "v_min_f32_e64 v115, -v115, 0\n"
        "v_min_f32 v132, 0, v116\n"
        "v_min_f32_e64 v116, -v116, 0\n"
        "v_min_f32 v133, 0, v117\n"
        "v_min_f32_e64 v117, -v117, 0\n"
        "v_min_f32 v134, 0, v118\n"
        "v_min_f32_e64 v118, -v118, 0\n"
        "v_min_f32 v135, 0, v119\n"
        "v_min_f32_e64 v119, -v119, 0\n"
        "v_lshlrev_b32 v31, 4, v0\n"
        "s_cmp_lt_u32 s59, 1\n"
        "s_cselect_b32 s98, 2147483648, 0\n"
        "v_or_b32 v29, s98, v31\n"
        "v_lshlrev_b32 v250, 3, v249\n"
        "v_lshl_add_u32 v4, v10, 15, v250\n"
        "s_lshl_b32 s98, s62, 8\n"
        "v_add_u32 v250, s98, v250\n"
        "v_mul_u32_u24 v251, 8192, v10\n"
        "v_add_u32 v230, v251, v250\n"
        "v_add_u32 v232, 16384, v250\n"
        "v_and_b32 v250, 3, v0\n"
        "v_and_b32 v251, 1, v14\n"
        "v_lshl_or_b32 v252, v251, 2, v250\n"
        "v_mul_u32_u24 v253, 4128, v252\n"
        "v_add_u32 v253, 20480, v253\n"
        "s_min_u32 s99, s59, 1\n"
        "s_lshl_b32 s99, s99, 11\n"
        "s_add_u32 s99, s99, 155840\n"
        "v_mov_b32 v254, s99\n"
        "v_cmp_lt_u32 vcc, 4, v252\n"
        "v_lshrrev_b32 v252, 2, v15\n"
        "v_lshl_add_u32 v255, v10, 2, v252\n"
        "v_cndmask_b32_e32 v253, v253, v254, vcc\n"
        "v_lshl_add_u32 v253, v255, 3, v253\n"
        "v_add_u32 v233, s98, v253\n"
        "s_lshl_b32 s98, s45, 8\n"
        "v_subrev_u32 v16, s98, v230\n"
        "v_subrev_u32 v17, s98, v232\n"
        "v_subrev_u32 v18, s98, v233\n"
        "v_add_u32 v231, 256, v16\n"
        "s_cmp_le_u32 s45, 0\n"
        "s_cselect_b32 s98, 1, 8\n"
        "s_lshl_b32 s98, s98, 8\n"
        "v_add_u32 v19, s98, v16\n"
        "v_add_u32 v22, s98, v17\n"
        "v_add_u32 v25, s98, v18\n"
        "s_cmp_le_u32 s45, 1\n"
        "s_cselect_b32 s98, 2, 9\n"
        "s_lshl_b32 s98, s98, 8\n"
        "v_add_u32 v20, s98, v16\n"
        "v_add_u32 v23, s98, v17\n"
        "v_add_u32 v26, s98, v18\n"
        "s_cmp_le_u32 s45, 2\n"
        "s_cselect_b32 s98, 3, 10\n"
        "s_lshl_b32 s98, s98, 8\n"
        "v_add_u32 v21, s98, v16\n"
        "v_add_u32 v24, s98, v17\n"
        "v_add_u32 v27, s98, v18\n"
        "v_subrev_u32 v253, 1, v10\n"
        "s_sub_u32 s98, s61, 1\n"
        "v_and_b32 v6, s98, v253\n"
        "v_xor_b32 v7, v6, v253\n"
        "v_mov_b32 v11, 0x207\n"
        "v_lshlrev_b32 v253, 4, v249\n"
        "v_lshl_add_u32 v8, v10, 15, v253\n"
        "v_lshlrev_b32 v253, 2, v249\n"
        "v_lshl_or_b32 v9, v10, 31, v253\n"
        "v_lshrrev_b32 v253, 3, v0\n"
        "v_bfe_u32 v254, v0, 1, 2\n"
        "s_lshl_b32 s98, s59, 1\n"
        "s_sub_u32 s98, s98, 2\n"
        "v_add_u32 v254, s98, v254\n"
        "v_cmp_le_i32 s[86:87], 0, v254\n"
        "v_lshl_add_u32 v254, s46, 4, v254\n"
        "s_lshl2_add_u32 s98, s61, s63\n"
        "v_and_b32 v255, 1, v0\n"
        "v_lshl_add_u32 v255, v255, 2, s98\n"
        "v_mul_u32_u24 v253, 144, v253\n"
        "v_add_u32 v255, v255, v253\n"
        "v_lshrrev_b32 v255, 3, v255\n"
        "v_lshl_add_u32 v254, v255, 5, v254\n"
        "v_mul_i32_i24 v254, 128, v254\n"
        "v_add_u32 v5, 12058624, v254\n"
        "s_and_b32 s86, s86, 0x00fffff0\n"
        "s_mov_b32 s87, 0\n"
        "s_and_b32 s97, s44, 7\n"
        "s_mul_i32 s97, s97, 144\n"
        "s_lshr_b32 s98, s44, 3\n"
        "s_lshl2_add_u32 s52, s43, s98\n"
        "s_lshl_b32 s98, s46, 5\n"
        "s_add_u32 s98, s98, s44\n"
        "s_cmp_lt_u32 s98, 27\n"
        "s_cselect_b32 s49, 1, 0\n"
        "s_cmp_eq_u32 s46, 0\n"
        "s_cbranch_scc1 .Lmk_g0c\n"
        "s_waitcnt vmcnt(0)\n"
        "v_mov_b32 v248, s40\n"
        "v_mov_b32 v249, s41\n"
        "s_mov_b64 exec, 1\n"
        "buffer_store_dwordx2 v[248:249], off, s[12:15], s56 sc1\n"
        "s_mov_b64 exec, -1\n"
        "s_branch .Lmk_g0d\n"
        ".Lmk_g0c:\n"
        "s_mul_i32 s98, s44, 57\n"
        "s_lshr_b32 s98, s98, 9\n"
        "s_mul_i32 s99, s98, 9\n"
        "s_sub_u32 s48, s44, s99\n"
        "s_mul_i32 s99, s43, 3\n"
        "s_add_u32 s47, s98, s99\n"
        "v_lshrrev_b32 v253, 3, v0\n"
        "v_and_b32 v254, 1, v14\n"
        "v_bfe_u32 v255, v0, 1, 2\n"
        "v_xor_b32 v254, v254, v255\n"
        "v_and_b32 v255, 1, v0\n"
        "v_lshl_or_b32 v254, v254, 1, v255\n"
        "v_mul_u32_u24 v253, 1152, v253\n"
        "v_xor_b32 v255, 4, v254\n"
        "v_lshl_add_u32 v246, v254, 4, v253\n"
        "v_lshl_add_u32 v247, v255, 4, v253\n"
        "v_add_u32 v247, 8192, v247\n"
        "v_mov_b32 v254, 155672\n"
        "s_mov_b32 s95, 1048576\n"
        "s_nop 0\n"
        ".Lmk_c8:\n"
        "ds_read_b32 v255, v254\n"
        "s_waitcnt lgkmcnt(0)\n"
        "v_readfirstlane_b32 s94, v255\n"
        "s_nop 0\n"
        "s_cmp_ge_u32 s94, 4\n"
        "s_cbranch_scc1 .Lmk_d9\n"
        "s_sub_u32 s95, s95, 1\n"
        "s_cbranch_scc0 .Lmk_c8\n"
        ".Lmk_d9:\n"
        "s_add_u32 s50, s54, 0\n"
        "s_add_u32 m0, s55, 131072\n"
        "s_nop 0\n"
        "buffer_load_dwordx4 v1, s[20:23], s50 offen lds\n"
        "s_add_u32 m0, s55, 131072\n"
        "s_nop 0\n"
        "buffer_load_dwordx4 v2, s[20:23], s50 offen offset:1024 lds\n"
        "s_add_u32 s50, s54, 589824\n"
        "s_add_u32 m0, s55, 139264\n"
        "s_nop 0\n"
        "buffer_load_dwordx4 v1, s[20:23], s50 offen lds\n"
        "s_add_u32 m0, s55, 139264\n"
        "s_nop 0\n"
        "buffer_load_dwordx4 v2, s[20:23], s50 offen offset:1024 lds\n"
        "s_add_u32 s50, s54, 1179648\n"
        "s_add_u32 m0, s55, 147456\n"
        "s_nop 0\n"
        "buffer_load_dwordx4 v1, s[20:23], s50 offen lds\n"
        "s_add_u32 m0, s55, 147456\n"
        "s_nop 0\n"
        "buffer_load_dwordx4 v2, s[20:23], s50 offen offset:1024 lds\n"
        "v_and_b32 v12, 31, v0\n"
        "v_and_b32 v13, 24, v0\n"
        "v_add_u32 v12, v12, v13\n"
        "s_and_b32 s93, s2, 15\n"
        "s_mul_i32 s93, s93, 4352\n"
        "v_lshlrev_b32 v12, 3, v12\n"
        "s_add_u32 s93, s93, 12321280\n"
        "buffer_load_dwordx2 v[12:13], v12, s[12:15], s93 offen sc1\n"
        "s_waitcnt vmcnt(0)\n"
        "v_cmp_ne_u32 vcc, s40, v13\n"
        "s_nop 0\n"
        "s_cmp_eq_u64 vcc, 0\n"
        "s_cbranch_scc0 .Lmk_wfspin\n"
        ".Lmk_wfok:\n"
        "v_mov_b32 v254, 155668\n"
        "v_mov_b32 v255, 1\n"
        "s_mov_b64 exec, 1\n"
        "ds_add_u32 v254, v255\n"
        "s_mov_b64 exec, -1\n"
        "s_setprio 1\n"
        ".Lmk_g0d:\n"
        "s_lshl2_add_u32 s98, s46, 155676\n"
        "s_cmp_eq_u32 s45, 3\n"
        "s_cbranch_scc0 .Lmk_s10\n"
        "s_cmp_eq_u32 s46, 1\n"
        "s_cbranch_scc0 .Lmk_s12\n"
        "s_sleep 12\n"
        ".Lmk_s12:\n"
        "s_mov_b64 exec, s[86:87]\n"
        "s_mov_b32 s95, 65536\n"
        ".Lmk_p13:\n"
        "buffer_load_dwordx2 v[248:249], v5, s[12:15], 0 offen sc1\n"
        "s_waitcnt vmcnt(0)\n"
        "v_cmp_ne_u64 vcc, s[40:41], v[248:249]\n"
        "s_nop 0\n"
        "s_cmp_eq_u64 vcc, 0\n"
        "s_cbranch_scc1 .Lmk_d14\n"
        "s_sleep 1\n"
        "s_sub_u32 s95, s95, 1\n"
        "s_cbranch_scc0 .Lmk_p13\n"
        ".Lmk_d14:\n"
        "s_mov_b64 exec, -1\n"
        ".Lmk_e11:\n"
        "v_mov_b32 v252, s98\n"
        "v_mov_b32 v253, 1\n"
        "s_mov_b64 exec, 1\n"
        "ds_add_u32 v252, v253\n"
        "s_mov_b64 exec, -1\n"
        ".Lmk_s10:\n"
        "v_mov_b32 v254, 155672\n"
        "s_mov_b32 s95, 1048576\n"
        "s_nop 0\n"
        ".Lmk_c15:\n"
        "ds_read_b32 v255, v254\n"
        "s_waitcnt lgkmcnt(0)\n"
        "v_readfirstlane_b32 s94, v255\n"
        "s_nop 0\n"
        "s_cmp_ge_u32 s94, 4\n"
        "s_cbranch_scc1 .Lmk_d16\n"
        "s_sub_u32 s95, s95, 1\n"
        "s_cbranch_scc0 .Lmk_c15\n"
        ".Lmk_d16:\n"
        "v_mov_b32 v254, s98\n"
        "s_mov_b32 s95, 1048576\n"
        "s_nop 0\n"
        ".Lmk_c17:\n"
        "ds_read_b32 v255, v254\n"
        "s_waitcnt lgkmcnt(0)\n"
        "v_readfirstlane_b32 s94, v255\n"
        "s_nop 0\n"
        "s_cmp_ge_u32 s94, 1\n"
        "s_cbranch_scc1 .Lmk_d18\n"
        "s_sub_u32 s95, s95, 1\n"
        "s_cbranch_scc0 .Lmk_c17\n"
        ".Lmk_d18:\n"
        "s_lshl1_add_u32 s99, s59, s46\n"
        "s_lshl_b32 s99, s99, 10\n"
        "s_add_u32 s99, s99, 1046528\n"
        "s_lshl_b32 s50, s46, 10\n"
        "s_add_u32 s92, s3, s45\n"
        "s_add_u32 s98, s92, 144\n"
        "s_mul_i32 s93, s45, 4096\n"
        "s_lshl_b32 s98, s98, 14\n"
        "s_add_u32 s98, s98, s99\n"
        "s_add_u32 m0, s93, s50\n"
        "s_nop 0\n"
        "buffer_load_dwordx4 v29, s[12:15], s98 offen sc1 lds\n"
        "buffer_load_dwordx4 v31, s[12:15], s98 offen offset:2048 sc1 lds\n"
        "s_lshl_b32 s98, s3, 14\n"
        "s_lshl_b32 s93, s100, 8\n"
        "s_add_u32 s93, s93, 1048576\n"
        "s_add_u32 s98, s98, s93\n"
        "buffer_load_dwordx2 v[224:225], v4, s[12:15], s98 offen sc1\n"
        "s_add_u32 s98, s98, 16384\n"
        "buffer_load_dwordx2 v[226:227], v4, s[12:15], s98 offen sc1\n"
        "s_add_u32 s98, s63, 4\n"
        "s_lshl_b32 s98, s98, 14\n"
        "s_add_u32 s98, s98, s93\n"
        "buffer_load_dwordx2 v[228:229], v4, s[12:15], s98 offen sc1\n"
        "s_add_u32 s98, s92, 288\n"
        "s_mul_i32 s93, s45, 4128\n"
        "s_add_u32 s93, s93, 20480\n"
        "s_lshl_b32 s98, s98, 14\n"
        "s_add_u32 s98, s98, s99\n"
        "s_add_u32 m0, s93, s50\n"
        "s_nop 0\n"
        "buffer_load_dwordx4 v29, s[12:15], s98 offen sc1 lds\n"
        "buffer_load_dwordx4 v31, s[12:15], s98 offen offset:2048 sc1 lds\n"
        "s_cmp_lt_u32 s45, 2\n"
        "s_cbranch_scc0 .Lmk_s19\n"
        "s_mul_i32 s98, s45, 144\n"
        "s_add_u32 s98, s98, s63\n"
        "s_add_u32 s98, s98, 148\n"
        "s_mul_i32 s93, s45, 20608\n"
        "s_add_u32 s93, s93, 16384\n"
        "s_lshl_b32 s98, s98, 14\n"
        "s_add_u32 s98, s98, s99\n"
        "s_add_u32 m0, s93, s50\n"
        "s_nop 0\n"
        "buffer_load_dwordx4 v29, s[12:15], s98 offen sc1 lds\n"
        "buffer_load_dwordx4 v31, s[12:15], s98 offen offset:2048 sc1 lds\n"
        ".Lmk_s19:\n"
        "s_cmp_eq_u32 s46, 0\n"
        "s_cbranch_scc0 .Lmk_s20\n"
        "v_lshrrev_b32 v252, 2, v15\n"
        "v_and_b32 v250, 3, v0\n"
        "v_lshrrev_b32 v251, 1, v252\n"
        "v_and_b32 v253, 1, v14\n"
        "v_lshl_or_b32 v251, v253, 1, v251\n"
        "v_xor_b32 v251, s45, v251\n"
        "v_lshl_add_u32 v253, v14, 3, v252\n"
        "v_lshlrev_b32 v254, 7, v253\n"
        "v_lshl_add_u32 v254, v251, 5, v254\n"
        "v_lshl_add_u32 v254, v250, 3, v254\n"
        "v_add_u32 v254, 106496, v254\n"
        "v_mov_b32 v248, 155668\n"
        "s_mov_b32 s95, 1048576\n"
        "s_nop 0\n"
        ".Lmk_c21:\n"
        "ds_read_b32 v249, v248\n"
        "s_waitcnt lgkmcnt(0)\n"
        "v_readfirstlane_b32 s94, v249\n"
        "s_nop 0\n"
        "s_cmp_ge_u32 s94, 4\n"
        "s_cbranch_scc1 .Lmk_d22\n"
        "s_sub_u32 s95, s95, 1\n"
        "s_cbranch_scc0 .Lmk_c21\n"
        ".Lmk_d22:\n"
        "ds_read_b64_tr_b16 v[32:33], v254 offset:0\n"
        "ds_read_b64_tr_b16 v[34:35], v254 offset:512\n"
        "ds_read_b64_tr_b16 v[36:37], v254 offset:4096\n"
        "ds_read_b64_tr_b16 v[38:39], v254 offset:4608\n"
        "ds_read_b64_tr_b16 v[40:41], v254 offset:8192\n"
        "ds_read_b64_tr_b16 v[42:43], v254 offset:8704\n"
        "ds_read_b64_tr_b16 v[44:45], v254 offset:12288\n"
        "ds_read_b64_tr_b16 v[46:47], v254 offset:12800\n"
        "ds_read_b64_tr_b16 v[48:49], v254 offset:16384\n"
        "ds_read_b64_tr_b16 v[50:51], v254 offset:16896\n"
        "ds_read_b64_tr_b16 v[52:53], v254 offset:20480\n"
        "ds_read_b64_tr_b16 v[54:55], v254 offset:20992\n"
        "ds_read_b64_tr_b16 v[56:57], v254 offset:24576\n"
        "ds_read_b64_tr_b16 v[58:59], v254 offset:25088\n"
        "ds_read_b64_tr_b16 v[60:61], v254 offset:28672\n"
        "ds_read_b64_tr_b16 v[62:63], v254 offset:29184\n"
        "ds_read_b64_tr_b16 v[64:65], v254 offset:32768\n"
        "ds_read_b64_tr_b16 v[66:67], v254 offset:33280\n"
        "ds_read_b64_tr_b16 v[68:69], v254 offset:36864\n"
        "ds_read_b64_tr_b16 v[70:71], v254 offset:37376\n"
        "ds_read_b64_tr_b16 v[72:73], v254 offset:40960\n"
        "ds_read_b64_tr_b16 v[74:75], v254 offset:41472\n"
        "ds_read_b64_tr_b16 v[76:77], v254 offset:45056\n"
        "ds_read_b64_tr_b16 v[78:79], v254 offset:45568\n"
        "s_lshl_b32 s98, s45, 8\n"
        "s_add_u32 s98, s98, 98304\n"
        "v_lshlrev_b32 v251, 4, v0\n"
        "v_add_u32 v251, s96, v251\n"
        "v_mov_b32 v243, s40\n"
        "v_mov_b32 v245, s40\n"
        "s_waitcnt lgkmcnt(15)\n"
        "v_mfma_f32_16x16x32_bf16 v[234:237], v[80:83], v[32:35], 0\n"
        "v_mfma_f32_16x16x32_bf16 v[234:237], v[84:87], v[36:39], v[234:237]\n"
        "v_mfma_f32_16x16x32_bf16 v[234:237], v[88:91], v[40:43], v[234:237]\n"
        "v_mfma_f32_16x16x32_bf16 v[234:237], v[92:95], v[44:47], v[234:237]\n"
        "s_waitcnt lgkmcnt(14)\n"
        "v_mfma_f32_16x16x32_bf16 v[234:237], v[96:99], v[48:51], v[234:237]\n"
        "s_waitcnt lgkmcnt(12)\n"
        "v_mfma_f32_16x16x32_bf16 v[234:237], v[100:103], v[52:55], v[234:237]\n"
        "s_waitcnt lgkmcnt(10)\n"
        "v_mfma_f32_16x16x32_bf16 v[238:241], v[80:83], v[56:59], 0\n"
        "s_waitcnt lgkmcnt(8)\n"
        "v_mfma_f32_16x16x32_bf16 v[238:241], v[84:87], v[60:63], v[238:241]\n"
        "s_waitcnt lgkmcnt(6)\n"
        "v_mfma_f32_16x16x32_bf16 v[238:241], v[88:91], v[64:67], v[238:241]\n"
        "s_waitcnt lgkmcnt(4)\n"
        "v_mfma_f32_16x16x32_bf16 v[238:241], v[92:95], v[68:71], v[238:241]\n"
        "s_waitcnt lgkmcnt(2)\n"
        "v_mfma_f32_16x16x32_bf16 v[238:241], v[96:99], v[72:75], v[238:241]\n"
        "s_waitcnt lgkmcnt(0)\n"
        "v_mfma_f32_16x16x32_bf16 v[238:241], v[100:103], v[76:79], v[238:241]\n"
        "v_min_u32 v252, 47, v0\n"
        "s_lshl2_add_u32 s99, s47, s45\n"
        "s_mul_i32 s99, s99, 48\n"
        "v_add_u32 v252, s99, v252\n"
        "v_lshrrev_b32 v253, 6, v252\n"
        "v_lshl_add_u32 v252, v253, 7, v252\n"
        "v_lshlrev_b32 v252, 4, v252\n"
        "v_add_u32 v28, 98304, v252\n"
        "v_mov_b32 v242, v234\n"
        "v_mov_b32 v244, v238\n"
        "s_mov_b64 exec, 0xffff\n"
        "buffer_store_dwordx4 v[242:245], v251, s[12:15], s98 offen sc1\n"
        "s_mov_b64 exec, -1\n"
        ".Lmk_s20:\n"
        "s_cmp_eq_u32 s46, 1\n"
        "s_cbranch_scc0 .Lmk_s23\n"
        "s_getpc_b64 s[90:91]\n"
        ".Lmk_pc2:\n"
        "s_add_u32 s90, s90, .Lmk_f0-.Lmk_pc2\n"
        "s_addc_u32 s91, s91, 0\n"
        "v_lshlrev_b32 v3, 6, v0\n"
        "v_min_u32 v3, .Lmk_f1-.Lmk_f0, v3\n"
        "global_load_dword v3, v3, s[90:91]\n"
        ".Lmk_s23:\n"
        "s_waitcnt vmcnt(1)\n"
        "s_lshl2_add_u32 s98, s46, 155648\n"
        "v_mov_b32 v254, s98\n"
        "v_mov_b32 v255, 1\n"
        "s_mov_b64 exec, 1\n"
        "ds_add_u32 v254, v255\n"
        "s_mov_b64 exec, -1\n"
        "v_and_b32 v228, v228, v6\n"
        "v_and_b32 v229, v229, v7\n"
        "s_mov_b32 s95, 1048576\n"
        "s_nop 0\n"
        ".Lmk_c24:\n"
        "ds_read_b32 v255, v254\n"
        "s_waitcnt lgkmcnt(0)\n"
        "v_readfirstlane_b32 s94, v255\n"
        "s_nop 0\n"
        "s_cmp_ge_u32 s94, 4\n"
        "s_cbranch_scc1 .Lmk_d25\n"
        "s_sub_u32 s95, s95, 1\n"
        "s_cbranch_scc0 .Lmk_c24\n"
        ".Lmk_d25:\n"
        "s_cmp_eq_u32 s46, 1\n"
        "s_cbranch_scc0 .Lmk_s26\n"
        "v_mov_b32 v254, 155648\n"
        "s_mov_b32 s95, 1048576\n"
        "s_nop 0\n"
        ".Lmk_c27:\n"
        "ds_read_b32 v255, v254\n"
        "s_waitcnt lgkmcnt(0)\n"
        "v_readfirstlane_b32 s94, v255\n"
        "s_nop 0\n"
        "s_cmp_ge_u32 s94, 4\n"
        "s_cbranch_scc1 .Lmk_d28\n"
        "s_sub_u32 s95, s95, 1\n"
        "s_cbranch_scc0 .Lmk_c27\n"
        ".Lmk_d28:\n"
        ".Lmk_s26:\n"
        "ds_read2st64_b64 v[192:195], v230 offset0:0 offset1:8\n"
        "ds_read2st64_b64 v[200:203], v232 offset0:0 offset1:4\n"
        "ds_read2st64_b64 v[196:199], v230 offset0:4 offset1:12\n"
        "ds_read_b64_tr_b16 v[208:209], v233 offset:0\n"
        "ds_read_b64_tr_b16 v[210:211], v233 offset:64\n"
        "ds_read_b64_tr_b16 v[212:213], v233 offset:128\n"
        "ds_read_b64_tr_b16 v[214:215], v233 offset:192\n"
        "s_waitcnt lgkmcnt(5)\n"
        "v_mfma_f32_32x32x16_bf16 v[136:151], v[192:195], v[224:227], v[104:119]\n"
        "v_mfma_f32_32x32x8_bf16 v[136:151], v[200:201], v[228:229], v[136:151]\n"
        "s_waitcnt lgkmcnt(0)\n"
        "v_mfma_f32_32x32x16_bf16 v[152:167], v[196:199], v[224:227], v[120:135]\n"
        "s_nop 9\n"
        "v_exp_f32 v136, v136\n"
        "v_exp_f32 v137, v137\n"
        "v_exp_f32 v138, v138\n"
        "v_exp_f32 v139, v139\n"
        "ds_read2st64_b64 v[192:195], v19 offset0:0 offset1:8\n"
        "v_exp_f32 v140, v140\n"
        "ds_read_b64 v[204:205], v22\n"
        "v_exp_f32 v141, v141\n"
        "v_mfma_f32_32x32x8_bf16 v[152:167], v[202:203], v[228:229], v[152:167]\n"
        "v_exp_f32 v142, v142\n"
        "ds_read_b64_tr_b16 v[216:217], v233 offset:2048\n"
        "v_exp_f32 v143, v143\n"
        "ds_read_b64_tr_b16 v[218:219], v233 offset:2112\n"
        "v_cvt_pk_bf16_f32 v184, v136, v137\n"
        "v_cvt_pk_bf16_f32 v185, v138, v139\n"
        "v_cvt_pk_bf16_f32 v186, v140, v141\n"
        "v_cvt_pk_bf16_f32 v187, v142, v143\n"
        "v_exp_f32 v144, v144\n"
        "v_exp_f32 v145, v145\n"
        "v_mfma_f32_32x32x16_bf16 v[168:183], v[208:211], v[184:187], 0\n"
        "v_exp_f32 v146, v146\n"
        "ds_read_b64_tr_b16 v[220:221], v233 offset:2176\n"
        "v_exp_f32 v147, v147\n"
        "ds_read_b64_tr_b16 v[222:223], v233 offset:2240\n"
        "v_exp_f32 v148, v148\n"
        "v_exp_f32 v149, v149\n"
        "v_exp_f32 v150, v150\n"
        "v_exp_f32 v151, v151\n"
        "s_cmp_eq_u32 s46, 1\n"
        "s_cbranch_scc0 .Lmk_s29\n"
        "s_mul_i32 s50, s97, 576\n"
        "s_lshl_b32 s51, s45, 10\n"
        "s_add_u32 s50, s50, 8388608\n"
        "s_add_u32 s50, s50, s51\n"
        "s_add_u32 m0, s51, 72704\n"
        "s_nop 0\n"
        "buffer_load_dwordx4 v31, s[12:15], s50 offen sc1 lds\n"
        "s_add_u32 s50, s50, 4096\n"
        "s_add_u32 m0, s51, 76800\n"
        "s_nop 0\n"
        "buffer_load_dwordx4 v31, s[12:15], s50 offen sc1 lds\n"
        ".Lmk_s29:\n"
        "v_cvt_pk_bf16_f32 v188, v144, v145\n"
        "v_cvt_pk_bf16_f32 v189, v146, v147\n"
        "v_cvt_pk_bf16_f32 v190, v148, v149\n"
        "v_cvt_pk_bf16_f32 v191, v150, v151\n"
        "s_waitcnt lgkmcnt(0)\n"
        "v_mfma_f32_32x32x16_bf16 v[136:151], v[192:195], v[224:227], 0\n"
        "v_exp_f32 v152, v152\n"
        "v_exp_f32 v153, v153\n"
        "v_exp_f32 v154, v154\n"
        "v_mfma_f32_32x32x16_bf16 v[168:183], v[212:215], v[188:191], v[168:183]\n"
        "v_exp_f32 v155, v155\n"
        "ds_read2st64_b64 v[196:199], v20 offset0:0 offset1:8\n"
        "v_exp_f32 v156, v156\n"
        "ds_read_b64 v[206:207], v23\n"
        "v_exp_f32 v157, v157\n"
        "v_mfma_f32_32x32x8_bf16 v[136:151], v[204:205], v[228:229], v[136:151]\n"
        "v_exp_f32 v158, v158\n"
        "ds_read_b64_tr_b16 v[208:209], v25 offset:0\n"
        "v_exp_f32 v159, v159\n"
        "ds_read_b64_tr_b16 v[210:211], v25 offset:64\n"
        "v_cvt_pk_bf16_f32 v184, v152, v153\n"
        "v_cvt_pk_bf16_f32 v185, v154, v155\n"
        "v_cvt_pk_bf16_f32 v186, v156, v157\n"
        "v_cvt_pk_bf16_f32 v187, v158, v159\n"
        "v_exp_f32 v160, v160\n"
        "v_exp_f32 v161, v161\n"
        "v_mfma_f32_32x32x16_bf16 v[168:183], v[216:219], v[184:187], v[168:183]\n"
        "v_exp_f32 v162, v162\n"
        "ds_read_b64_tr_b16 v[212:213], v25 offset:128\n"
        "v_exp_f32 v163, v163\n"
        "ds_read_b64_tr_b16 v[214:215], v25 offset:192\n"
        "v_exp_f32 v164, v164\n"
        "v_exp_f32 v165, v165\n"
        "v_exp_f32 v166, v166\n"
        "v_exp_f32 v167, v167\n"
        "s_cmp_eq_u32 s46, 0\n"
        "s_cbranch_scc0 .Lmk_s30\n"
        "s_mul_i32 s50, s47, 12\n"
        "s_add_u32 s50, s50, s45\n"
        "s_mul_i32 s50, s50, 18432\n"
        "s_lshl_b32 s51, s48, 7\n"
        "s_add_u32 s50, s50, s51\n"
        "s_cmp_lt_u32 s44, 27\n"
        "s_cselect_b32 s50, s50, 0x40000000\n"
        "s_add_u32 m0, s55, 41216\n"
        "s_nop 0\n"
        "buffer_load_dwordx4 v246, s[24:27], s50 offen lds\n"
        "s_add_u32 m0, s55, 41216\n"
        "s_nop 0\n"
        "buffer_load_dwordx4 v247, s[24:27], s50 offen offset:1024 lds\n"
        ".Lmk_s30:\n"
        "s_cmp_eq_u32 s46, 1\n"
        "s_cbranch_scc0 .Lmk_s31\n"
        "s_add_u32 s50, s50, 4096\n"
        "s_add_u32 m0, s51, 80896\n"
        "s_nop 0\n"
        "buffer_load_dwordx4 v31, s[12:15], s50 offen sc1 lds\n"
        "s_add_u32 s50, s50, 4096\n"
        "s_add_u32 m0, s51, 84992\n"
        "s_nop 0\n"
        "buffer_load_dwordx4 v31, s[12:15], s50 offen sc1 lds\n"
        ".Lmk_s31:\n"
        "v_cvt_pk_bf16_f32 v188, v160, v161\n"
        "v_cvt_pk_bf16_f32 v189, v162, v163\n"
        "v_cvt_pk_bf16_f32 v190, v164, v165\n"
        "v_cvt_pk_bf16_f32 v191, v166, v167\n"
        "s_waitcnt lgkmcnt(0)\n"
        "v_mfma_f32_32x32x16_bf16 v[152:167], v[196:199], v[224:227], 0\n"
        "v_exp_f32 v136, v136\n"
        "v_exp_f32 v137, v137\n"
        "v_exp_f32 v138, v138\n"
        "v_mfma_f32_32x32x16_bf16 v[168:183], v[220:223], v[188:191], v[168:183]\n"
        "v_exp_f32 v139, v139\n"
        "ds_read2st64_b64 v[192:195], v21 offset0:0 offset1:8\n"
        "v_exp_f32 v140, v140\n"
        "ds_read_b64 v[200:201], v24\n"
        "v_exp_f32 v141, v141\n"
        "v_mfma_f32_32x32x8_bf16 v[152:167], v[206:207], v[228:229], v[152:167]\n"
        "v_exp_f32 v142, v142\n"
        "ds_read_b64_tr_b16 v[216:217], v26 offset:0\n"
        "v_exp_f32 v143, v143\n"
        "ds_read_b64_tr_b16 v[218:219], v26 offset:64\n"
        "v_cvt_pk_bf16_f32 v184, v136, v137\n"
        "v_cvt_pk_bf16_f32 v185, v138, v139\n"
        "v_cvt_pk_bf16_f32 v186, v140, v141\n"
        "v_cvt_pk_bf16_f32 v187, v142, v143\n"
        "v_exp_f32 v144, v144\n"
        "v_exp_f32 v145, v145\n"
        "v_mfma_f32_32x32x16_bf16 v[168:183], v[208:211], v[184:187], v[168:183]\n"
        "v_exp_f32 v146, v146\n"
        "ds_read_b64_tr_b16 v[220:221], v26 offset:128\n"
        "v_exp_f32 v147, v147\n"
        "ds_read_b64_tr_b16 v[222:223], v26 offset:192\n"
        "v_exp_f32 v148, v148\n"
        "v_exp_f32 v149, v149\n"
        "v_exp_f32 v150, v150\n"
        "v_exp_f32 v151, v151\n"
        "s_cmp_eq_u32 s46, 0\n"
        "s_cbranch_scc0 .Lmk_s32\n"
        "s_add_u32 s50, s50, 73728\n"
        "s_add_u32 m0, s55, 49408\n"
        "s_nop 0\n"
        "buffer_load_dwordx4 v246, s[24:27], s50 offen lds\n"
        "s_add_u32 m0, s55, 49408\n"
        "s_nop 0\n"
        "buffer_load_dwordx4 v247, s[24:27], s50 offen offset:1024 lds\n"
        ".Lmk_s32:\n"
        "s_cmp_eq_u32 s46, 1\n"
        "s_cbranch_scc0 .Lmk_s33\n"
        "s_add_u32 s50, s50, 4096\n"
        "s_add_u32 m0, s51, 89088\n"
        "s_nop 0\n"
        "buffer_load_dwordx4 v31, s[12:15], s50 offen sc1 lds\n"
        "s_add_u32 s50, s50, 4096\n"
        "s_add_u32 m0, s51, 93184\n"
        "s_nop 0\n"
        "buffer_load_dwordx4 v31, s[12:15], s50 offen sc1 lds\n"
        ".Lmk_s33:\n"
        "v_cvt_pk_bf16_f32 v188, v144, v145\n"
        "v_cvt_pk_bf16_f32 v189, v146, v147\n"
        "v_cvt_pk_bf16_f32 v190, v148, v149\n"
        "v_cvt_pk_bf16_f32 v191, v150, v151\n"
        "s_waitcnt lgkmcnt(0)\n"
        "v_mfma_f32_32x32x16_bf16 v[136:151], v[192:195], v[224:227], 0\n"
        "v_exp_f32 v152, v152\n"
        "v_exp_f32 v153, v153\n"
        "v_exp_f32 v154, v154\n"
        "v_mfma_f32_32x32x16_bf16 v[168:183], v[212:215], v[188:191], v[168:183]\n"
        "v_exp_f32 v155, v155\n"
        "ds_read_b64_tr_b16 v[208:209], v27 offset:0\n"
        "v_exp_f32 v156, v156\n"
        "ds_read_b64_tr_b16 v[210:211], v27 offset:64\n"
        "v_exp_f32 v157, v157\n"
        "v_mfma_f32_32x32x8_bf16 v[136:151], v[200:201], v[228:229], v[136:151]\n"
        "v_exp_f32 v158, v158\n"
        "v_exp_f32 v159, v159\n"
        "v_cvt_pk_bf16_f32 v184, v152, v153\n"
        "v_cvt_pk_bf16_f32 v185, v154, v155\n"
        "v_cvt_pk_bf16_f32 v186, v156, v157\n"
        "v_cvt_pk_bf16_f32 v187, v158, v159\n"
        "v_exp_f32 v160, v160\n"
        "v_exp_f32 v161, v161\n"
        "v_mfma_f32_32x32x16_bf16 v[168:183], v[216:219], v[184:187], v[168:183]\n"
        "v_exp_f32 v162, v162\n"
        "ds_read_b64_tr_b16 v[212:213], v27 offset:128\n"
        "v_exp_f32 v163, v163\n"
        "ds_read_b64_tr_b16 v[214:215], v27 offset:192\n"
        "v_exp_f32 v164, v164\n"
        "v_exp_f32 v165, v165\n"
        "v_exp_f32 v166, v166\n"
        "v_exp_f32 v167, v167\n"
        "s_cmp_eq_u32 s46, 0\n"
        "s_cbranch_scc0 .Lmk_s34\n"
        "s_add_u32 s50, s50, 73728\n"
        "s_add_u32 m0, s55, 57600\n"
        "s_nop 0\n"
        "buffer_load_dwordx4 v246, s[24:27], s50 offen lds\n"
        "s_add_u32 m0, s55, 57600\n"
        "s_nop 0\n"
        "buffer_load_dwordx4 v247, s[24:27], s50 offen offset:1024 lds\n"
        ".Lmk_s34:\n"
        "s_cmp_eq_u32 s46, 1\n"
        "s_cbranch_scc0 .Lmk_s35\n"
        "s_add_u32 s50, s50, 4096\n"
        "s_add_u32 m0, s51, 97280\n"
        "s_nop 0\n"
        "buffer_load_dwordx4 v31, s[12:15], s50 offen sc1 lds\n"
        "s_add_u32 s50, s50, 4096\n"
        "s_add_u32 m0, s51, 101376\n"
        "s_nop 0\n"
        "buffer_load_dwordx4 v31, s[12:15], s50 offen sc1 lds\n"
        ".Lmk_s35:\n"
        "v_cvt_pk_bf16_f32 v188, v160, v161\n"
        "v_cvt_pk_bf16_f32 v189, v162, v163\n"
        "v_cvt_pk_bf16_f32 v190, v164, v165\n"
        "v_cvt_pk_bf16_f32 v191, v166, v167\n"
        "s_waitcnt lgkmcnt(0)\n"
        "s_cmp_eq_u32 s46, 0\n"
        "s_cbranch_scc0 .Lmk_s36\n"
        "v_mov_b32 v254, 155652\n"
        "s_mov_b32 s95, 1048576\n"
        "s_nop 0\n"
        ".Lmk_c37:\n"
        "ds_read_b32 v255, v254\n"
        "s_waitcnt lgkmcnt(0)\n"
        "v_readfirstlane_b32 s94, v255\n"
        "s_nop 0\n"
        "s_cmp_ge_u32 s94, 4\n"
        "s_cbranch_scc1 .Lmk_d38\n"
        "s_sub_u32 s95, s95, 1\n"
        "s_cbranch_scc0 .Lmk_c37\n"
        ".Lmk_d38:\n"
        ".Lmk_s36:\n"
        "ds_read2st64_b64 v[196:199], v16 offset0:2 offset1:10\n"
        "ds_read2_b64 v[204:207], v17 offset0:128 offset1:160\n"
        "v_exp_f32 v136, v136\n"
        "ds_read2st64_b64 v[192:195], v231 offset0:2 offset1:10\n"
        "v_exp_f32 v137, v137\n"
        "ds_read_b64_tr_b16 v[216:217], v18 offset:1024\n"
        "v_exp_f32 v138, v138\n"
        "v_mfma_f32_32x32x16_bf16 v[168:183], v[220:223], v[188:191], v[168:183]\n"
        "v_exp_f32 v139, v139\n"
        "ds_read_b64_tr_b16 v[218:219], v18 offset:1088\n"
        "v_exp_f32 v140, v140\n"
        "v_exp_f32 v141, v141\n"
        "v_exp_f32 v142, v142\n"
        "s_waitcnt lgkmcnt(3)\n"
        "v_mfma_f32_32x32x16_bf16 v[152:167], v[196:199], v[224:227], 0\n"
        "v_exp_f32 v143, v143\n"
        "v_cvt_pk_bf16_f32 v184, v136, v137\n"
        "v_cvt_pk_bf16_f32 v185, v138, v139\n"
        "v_cvt_pk_bf16_f32 v186, v140, v141\n"
        "v_cvt_pk_bf16_f32 v187, v142, v143\n"
        "v_exp_f32 v144, v144\n"
        "v_exp_f32 v145, v145\n"
        "v_mfma_f32_32x32x16_bf16 v[168:183], v[208:211], v[184:187], v[168:183]\n"
        "v_exp_f32 v146, v146\n"
        "ds_read_b64_tr_b16 v[220:221], v18 offset:1152\n"
        "v_exp_f32 v147, v147\n"
        "ds_read_b64_tr_b16 v[222:223], v18 offset:1216\n"
        "v_exp_f32 v148, v148\n"
        "v_mfma_f32_32x32x8_bf16 v[152:167], v[204:205], v[228:229], v[152:167]\n"
        "v_exp_f32 v149, v149\n"
        "v_exp_f32 v150, v150\n"
        "v_exp_f32 v151, v151\n"
        "s_cmp_eq_u32 s46, 0\n"
        "s_cbranch_scc0 .Lmk_s39\n"
        "s_mul_i32 s50, s97, 576\n"
        "s_lshl_b32 s51, s45, 10\n"
        "s_add_u32 s50, s50, 8437760\n"
        "s_add_u32 s50, s50, s51\n"
        "s_add_u32 m0, s51, 121856\n"
        "s_nop 0\n"
        "buffer_load_dwordx4 v31, s[12:15], s50 offen sc1 lds\n"
        "s_add_u32 s50, s50, 4096\n"
        "s_add_u32 m0, s51, 125952\n"
        "s_nop 0\n"
        "buffer_load_dwordx4 v31, s[12:15], s50 offen sc1 lds\n"
        "s_add_u32 s50, s50, 4096\n"
        "s_add_u32 m0, s51, 130048\n"
        "s_nop 0\n"
        "buffer_load_dwordx4 v31, s[12:15], s50 offen sc1 lds\n"
        ".Lmk_s39:\n"
        "s_cmp_eq_u32 s46, 1\n"
        "s_cbranch_scc0 .Lmk_s40\n"
        "s_add_u32 s50, s50, 4096\n"
        "s_add_u32 m0, s51, 105472\n"
        "s_nop 0\n"
        "buffer_load_dwordx4 v31, s[12:15], s50 offen sc1 lds\n"
        "s_add_u32 s50, s50, 4096\n"
        "s_add_u32 m0, s51, 109568\n"
        "s_nop 0\n"
        "buffer_load_dwordx4 v31, s[12:15], s50 offen sc1 lds\n"
        ".Lmk_s40:\n"
        "v_cvt_pk_bf16_f32 v188, v144, v145\n"
        "v_cvt_pk_bf16_f32 v189, v146, v147\n"
        "v_cvt_pk_bf16_f32 v190, v148, v149\n"
        "v_cvt_pk_bf16_f32 v191, v150, v151\n"
        "s_waitcnt lgkmcnt(0)\n"
        "v_mfma_f32_32x32x16_bf16 v[136:151], v[192:195], v[224:227], 0\n"
        "v_exp_f32 v152, v152\n"
        "v_exp_f32 v153, v153\n"
        "v_exp_f32 v154, v154\n"
        "v_mfma_f32_32x32x16_bf16 v[168:183], v[212:215], v[188:191], v[168:183]\n"
        "v_exp_f32 v155, v155\n"
        "ds_read2st64_b64 v[196:199], v16 offset0:3 offset1:11\n"
        "v_exp_f32 v156, v156\n"
        "ds_read2_b64 v[200:203], v17 offset0:192 offset1:224\n"
        "v_exp_f32 v157, v157\n"
        "v_mfma_f32_32x32x8_bf16 v[136:151], v[206:207], v[228:229], v[136:151]\n"
        "v_exp_f32 v158, v158\n"
        "ds_read_b64_tr_b16 v[208:209], v18 offset:1280\n"
        "v_exp_f32 v159, v159\n"
        "ds_read_b64_tr_b16 v[210:211], v18 offset:1344\n"
        "v_cvt_pk_bf16_f32 v184, v152, v153\n"
        "v_cvt_pk_bf16_f32 v185, v154, v155\n"
        "v_cvt_pk_bf16_f32 v186, v156, v157\n"
        "v_cvt_pk_bf16_f32 v187, v158, v159\n"
        "v_exp_f32 v160, v160\n"
        "v_exp_f32 v161, v161\n"
        "v_mfma_f32_32x32x16_bf16 v[168:183], v[216:219], v[184:187], v[168:183]\n"
        "v_exp_f32 v162, v162\n"
        "ds_read_b64_tr_b16 v[212:213], v18 offset:1408\n"
        "v_exp_f32 v163, v163\n"
        "ds_read_b64_tr_b16 v[214:215], v18 offset:1472\n"
        "v_exp_f32 v164, v164\n"
        "v_exp_f32 v165, v165\n"
        "v_exp_f32 v166, v166\n"
        "v_exp_f32 v167, v167\n"
        "s_cmp_eq_u32 s46, 0\n"
        "s_cbranch_scc0 .Lmk_s41\n"
        "s_add_u32 s50, s50, 4096\n"
        "s_add_u32 m0, s51, 134144\n"
        "s_nop 0\n"
        "buffer_load_dwordx4 v31, s[12:15], s50 offen sc1 lds\n"
        "s_add_u32 s50, s50, 4096\n"
        "s_add_u32 m0, s51, 138240\n"
        "s_nop 0\n"
        "buffer_load_dwordx4 v31, s[12:15], s50 offen sc1 lds\n"
        "s_add_u32 s50, s50, 4096\n"
        "s_add_u32 m0, s51, 142336\n"
        "s_nop 0\n"
        "buffer_load_dwordx4 v31, s[12:15], s50 offen sc1 lds\n"
        ".Lmk_s41:\n"
        "s_cmp_eq_u32 s46, 1\n"
        "s_cbranch_scc0 .Lmk_s42\n"
        "s_add_u32 s50, s50, 4096\n"
        "s_add_u32 m0, s51, 113664\n"
        "s_nop 0\n"
        "buffer_load_dwordx4 v31, s[12:15], s50 offen sc1 lds\n"
        "s_add_u32 s50, s50, 4096\n"
        "s_add_u32 m0, s51, 117760\n"
        "s_nop 0\n"
        "buffer_load_dwordx4 v31, s[12:15], s50 offen sc1 lds\n"
        ".Lmk_s42:\n"
        "v_cvt_pk_bf16_f32 v188, v160, v161\n"
        "v_cvt_pk_bf16_f32 v189, v162, v163\n"
        "v_cvt_pk_bf16_f32 v190, v164, v165\n"
        "v_cvt_pk_bf16_f32 v191, v166, v167\n"
        "s_waitcnt lgkmcnt(0)\n"
        "v_mfma_f32_32x32x16_bf16 v[152:167], v[196:199], v[224:227], 0\n"
        "v_exp_f32 v136, v136\n"
        "v_exp_f32 v137, v137\n"
        "v_exp_f32 v138, v138\n"
        "v_mfma_f32_32x32x16_bf16 v[168:183], v[220:223], v[188:191], v[168:183]\n"
        "v_exp_f32 v139, v139\n"
        "ds_read2st64_b64 v[192:195], v231 offset0:3 offset1:11\n"
        "v_exp_f32 v140, v140\n"
        "ds_read_b64_tr_b16 v[216:217], v18 offset:1536\n"
        "v_exp_f32 v141, v141\n"
        "v_mfma_f32_32x32x8_bf16 v[152:167], v[200:201], v[228:229], v[152:167]\n"
        "v_exp_f32 v142, v142\n"
        "ds_read_b64_tr_b16 v[218:219], v18 offset:1600\n"
        "v_exp_f32 v143, v143\n"
        "v_cvt_pk_bf16_f32 v184, v136, v137\n"
        "v_cvt_pk_bf16_f32 v185, v138, v139\n"
        "v_cvt_pk_bf16_f32 v186, v140, v141\n"
        "v_cvt_pk_bf16_f32 v187, v142, v143\n"
        "v_exp_f32 v144, v144\n"
        "v_exp_f32 v145, v145\n"
        "v_mfma_f32_32x32x16_bf16 v[168:183], v[208:211], v[184:187], v[168:183]\n"
        "v_exp_f32 v146, v146\n"
        "ds_read_b64_tr_b16 v[220:221], v18 offset:1664\n"
        "v_exp_f32 v147, v147\n"
        "ds_read_b64_tr_b16 v[222:223], v18 offset:1728\n"
        "v_exp_f32 v148, v148\n"
        "v_exp_f32 v149, v149\n"
        "v_exp_f32 v150, v150\n"
        "v_exp_f32 v151, v151\n"
        "s_cmp_eq_u32 s46, 0\n"
        "s_cbranch_scc0 .Lmk_s43\n"
        "s_add_u32 s50, s50, 4096\n"
        "s_add_u32 m0, s51, 146432\n"
        "s_nop 0\n"
        "buffer_load_dwordx4 v31, s[12:15], s50 offen sc1 lds\n"
        "s_add_u32 s50, s50, 4096\n"
        "s_add_u32 m0, s51, 150528\n"
        "s_nop 0\n"
        "buffer_load_dwordx4 v31, s[12:15], s50 offen sc1 lds\n"
        "s_add_u32 s50, s50, 4096\n"
        "s_cmp_eq_u32 s45, 0\n"
        "s_cbranch_scc0 .Lmk_s44\n"
        "s_mov_b32 m0, 154624\n"
        "s_nop 0\n"
        "buffer_load_dwordx4 v31, s[12:15], s50 offen sc1 lds\n"
        ".Lmk_s44:\n"
        ".Lmk_s43:\n"
        "v_cvt_pk_bf16_f32 v188, v144, v145\n"
        "v_cvt_pk_bf16_f32 v189, v146, v147\n"
        "v_cvt_pk_bf16_f32 v190, v148, v149\n"
        "v_cvt_pk_bf16_f32 v191, v150, v151\n"
        "s_waitcnt lgkmcnt(0)\n"
        "v_mfma_f32_32x32x16_bf16 v[136:151], v[192:195], v[224:227], 0\n"
        "v_exp_f32 v152, v152\n"
        "v_exp_f32 v153, v153\n"
        "v_exp_f32 v154, v154\n"
        "v_mfma_f32_32x32x16_bf16 v[168:183], v[212:215], v[188:191], v[168:183]\n"
        "v_exp_f32 v155, v155\n"
        "ds_read_b64_tr_b16 v[208:209], v18 offset:1792\n"
        "v_exp_f32 v156, v156\n"
        "ds_read_b64_tr_b16 v[210:211], v18 offset:1856\n"
        "v_exp_f32 v157, v157\n"
        "v_mfma_f32_32x32x8_bf16 v[136:151], v[202:203], v[228:229], v[136:151]\n"
        "v_exp_f32 v158, v158\n"
        "v_exp_f32 v159, v159\n"
        "v_cvt_pk_bf16_f32 v184, v152, v153\n"
        "v_cvt_pk_bf16_f32 v185, v154, v155\n"
        "v_cvt_pk_bf16_f32 v186, v156, v157\n"
        "v_cvt_pk_bf16_f32 v187, v158, v159\n"
        "v_exp_f32 v160, v160\n"
        "v_exp_f32 v161, v161\n"
        "v_mfma_f32_32x32x16_bf16 v[168:183], v[216:219], v[184:187], v[168:183]\n"
        "v_exp_f32 v162, v162\n"
        "ds_read_b64_tr_b16 v[212:213], v18 offset:1920\n"
        "v_exp_f32 v163, v163\n"
        "ds_read_b64_tr_b16 v[214:215], v18 offset:1984\n"
        "v_exp_f32 v164, v164\n"
        "v_exp_f32 v165, v165\n"
        "v_exp_f32 v166, v166\n"
        "v_exp_f32 v167, v167\n"
        "s_cmp_eq_u32 s46, 1\n"
        "s_cbranch_scc0 .Lmk_s45\n"
        "s_waitcnt vmcnt(0)\n"
        "v_mov_b32 v254, 155688\n"
        "v_mov_b32 v255, 1\n"
        "s_mov_b64 exec, 1\n"
        "ds_add_u32 v254, v255\n"
        "s_mov_b64 exec, -1\n"
        ".Lmk_s45:\n"
        "v_cvt_pk_bf16_f32 v188, v160, v161\n"
        "v_cvt_pk_bf16_f32 v189, v162, v163\n"
        "v_cvt_pk_bf16_f32 v190, v164, v165\n"
        "v_cvt_pk_bf16_f32 v191, v166, v167\n"
        "s_waitcnt lgkmcnt(0)\n"
        "v_exp_f32 v136, v136\n"
        "v_exp_f32 v137, v137\n"
        "v_exp_f32 v138, v138\n"
        "v_mfma_f32_32x32x16_bf16 v[168:183], v[220:223], v[188:191], v[168:183]\n"
        "v_exp_f32 v139, v139\n"
        "v_exp_f32 v140, v140\n"
        "v_exp_f32 v141, v141\n"
        "v_exp_f32 v142, v142\n"
        "v_exp_f32 v143, v143\n"
        "v_cvt_pk_bf16_f32 v184, v136, v137\n"
        "v_cvt_pk_bf16_f32 v185, v138, v139\n"
        "v_cvt_pk_bf16_f32 v186, v140, v141\n"
        "v_cvt_pk_bf16_f32 v187, v142, v143\n"
        "v_exp_f32 v144, v144\n"
        "v_exp_f32 v145, v145\n"
        "v_mfma_f32_32x32x16_bf16 v[168:183], v[208:211], v[184:187], v[168:183]\n"
        "v_exp_f32 v146, v146\n"
        "v_exp_f32 v147, v147\n"
        "v_exp_f32 v148, v148\n"
        "v_exp_f32 v149, v149\n"
        "v_exp_f32 v150, v150\n"
        "v_exp_f32 v151, v151\n"
        "s_cmp_eq_u32 s49, 1\n"
        "s_cbranch_scc0 .Lmk_s46\n"
        "buffer_load_dwordx4 v[234:237], v28, s[12:15], 0 offen offset:0 sc1\n"
        "buffer_load_dwordx4 v[238:241], v28, s[12:15], 0 offen offset:1024 sc1\n"
        "buffer_load_dwordx4 v[242:245], v28, s[12:15], 0 offen offset:2048 sc1\n"
        ".Lmk_s46:\n"
        "v_cvt_pk_bf16_f32 v188, v144, v145\n"
        "v_cvt_pk_bf16_f32 v189, v146, v147\n"
        "v_cvt_pk_bf16_f32 v190, v148, v149\n"
        "v_cvt_pk_bf16_f32 v191, v150, v151\n"
        "s_lshl_b32 s98, s60, 16\n"
        "s_nop 0\n"
        "v_mfma_f32_32x32x16_bf16 v[168:183], v[212:215], v[188:191], v[168:183]\n"
        "s_lshl_b32 s99, s100, 9\n"
        "s_add_u32 s98, s98, s99\n"
        "s_add_u32 s98, s98, 9437184\n"
        "s_lshr_b32 s99, s60, 0\n"
        "s_lshl_b32 s99, s99, 13\n"
        "s_lshl_b32 s92, s100, 7\n"
        "s_add_u32 s99, s99, s92\n"
        "s_and_b32 s92, s60, 0\n"
        "s_lshl2_add_u32 s99, s92, s99\n"
        "s_add_u32 s99, s99, 11534336\n"
        "s_cmp_eq_u32 s61, 1\n"
        "s_cselect_b64 s[88:89], -1, 0\n"
        "s_mov_b32 s92, 0x2b800000\n"
        "s_mov_b32 s93, 0x5d800000\n"
        "v_cmp_nle_f32 vcc, s92, v180\n"
        "v_cmp_nge_f32 s[90:91], s93, v180\n"
        "v_pk_add_f32 v[248:249], v[168:169], v[170:171]\n"
        "v_pk_add_f32 v[250:251], v[172:173], v[174:175]\n"
        "v_pk_add_f32 v[252:253], v[176:177], v[178:179]\n"
        "s_or_b64 s[90:91], s[90:91], vcc\n"
        "v_pk_add_f32 v[248:249], v[248:249], v[250:251]\n"
        "v_pk_add_f32 v[248:249], v[248:249], v[252:253]\n"
        "v_add_f32 v248, v248, v249\n"
        "v_cmp_class_f32 vcc, v248, v11\n"
        "s_nop 0\n"
        "s_or_b64 vcc, vcc, s[90:91]\n"
        "s_cbranch_scc1 .Lmk_slow\n"
        ".Lmk_norm:\n"
        "v_rcp_f32 v248, v180\n"
        "v_cndmask_b32_e64 v176, v176, v178, s[88:89]\n"
        "v_cndmask_b32_e64 v177, v177, v179, s[88:89]\n"
        "v_mul_f32 v168, v168, v248\n"
        "v_mul_f32 v169, v169, v248\n"
        "v_mul_f32 v170, v170, v248\n"
        "v_mul_f32 v171, v171, v248\n"
        "v_mul_f32 v172, v172, v248\n"
        "v_mul_f32 v173, v173, v248\n"
        "v_mul_f32 v174, v174, v248\n"
        "v_mul_f32 v175, v175, v248\n"
        "v_mul_f32 v176, v176, v248\n"
        "v_mul_f32 v177, v177, v248\n"
        "v_cvt_pk_bf16_f32 v184, v168, v169\n"
        "v_cvt_pk_bf16_f32 v185, v170, v171\n"
        "v_cvt_pk_bf16_f32 v186, v172, v173\n"
        "v_cvt_pk_bf16_f32 v187, v174, v175\n"
        "v_cvt_pk_bf16_f32 v188, v176, v177\n"
        "buffer_store_dwordx4 v[184:187], v8, s[12:15], s98 offen sc1\n"
        "buffer_store_dword v188, v9, s[12:15], s99 offen sc1\n"
        "s_lshl1_add_u32 s63, s52, s46\n"
        "s_lshr_b32 s3, s45, 1\n"
        "s_and_b32 s98, s60, 3\n"
        "s_lshl2_add_u32 s98, s100, s98\n"
        "s_lshl4_add_u32 s98, s98, s43\n"
        "s_lshl3_add_u32 s56, s98, 655360\n"
        "v_mov_b32 v254, 155692\n"
        "v_mov_b32 v255, 1\n"
        "s_mov_b64 exec, 1\n"
        "ds_add_u32 v254, v255\n"
        "s_mov_b64 exec, -1\n"
        "s_cmp_eq_u32 s46, 0\n"
        "s_cbranch_scc0 .Lmk_g147\n"
        "s_waitcnt vmcnt(0)\n"
        "v_mov_b32 v250, s40\n"
        "v_mov_b32 v251, s41\n"
        "s_mov_b64 exec, 1\n"
        "buffer_store_dwordx2 v[250:251], off, s[12:15], s56 sc1\n"
        "s_mov_b64 exec, -1\n"
        "s_cmp_eq_u32 s49, 1\n"
        "s_cbranch_scc0 .Lmk_n49\n"
        "s_mov_b32 s95, 65536\n"
        ".Lmk_hre:\n"
        "v_cmp_ne_u32 vcc, s40, v235\n"
        "v_cmp_ne_u32 s[90:91], s40, v237\n"
        "s_nop 0\n"
        "s_or_b64 vcc, vcc, s[90:91]\n"
        "v_cmp_ne_u32 s[90:91], s40, v239\n"
        "s_nop 0\n"
        "s_or_b64 vcc, vcc, s[90:91]\n"
        "v_cmp_ne_u32 s[90:91], s40, v241\n"
        "s_nop 0\n"
        "s_or_b64 vcc, vcc, s[90:91]\n"
        "v_cmp_ne_u32 s[90:91], s40, v243\n"
        "s_nop 0\n"
        "s_or_b64 vcc, vcc, s[90:91]\n"
        "v_cmp_ne_u32 s[90:91], s40, v245\n"
        "s_nop 0\n"
        "s_or_b64 vcc, vcc, s[90:91]\n"
        "s_cbranch_scc0 .Lmk_hok\n"
        "s_sleep 1\n"
        "buffer_load_dwordx4 v[234:237], v28, s[12:15], 0 offen offset:0 sc1\n"
        "buffer_load_dwordx4 v[238:241], v28, s[12:15], 0 offen offset:1024 sc1\n"
        "buffer_load_dwordx4 v[242:245], v28, s[12:15], 0 offen offset:2048 sc1\n"
        "s_waitcnt vmcnt(0)\n"
        "s_sub_u32 s95, s95, 1\n"
        "s_cbranch_scc0 .Lmk_hre\n"
        ".Lmk_hok:\n"
        "v_add_f32 v248, v234, v238\n"
        "v_add_f32 v249, v236, v240\n"
        "v_add_f32 v248, v248, v242\n"
        "v_add_f32 v249, v249, v244\n"
        "v_cvt_pk_bf16_f32 v248, v248, v248\n"
        "v_lshlrev_b32 v248, 16, v248\n"
        "v_cvt_pk_bf16_f32 v249, v249, v249\n"
        "v_lshlrev_b32 v249, 16, v249\n"
        "v_mul_f32 v250, 0xbfb8aa3b, v248\n"
        "v_exp_f32 v250, v250\n"
        "s_nop 0\n"
        "v_add_f32 v250, 1.0, v250\n"
        "v_rcp_f32 v250, v250\n"
        "s_mul_i32 s98, s45, 96\n"
        "s_add_u32 s98, s98, 162048\n"
        "v_mul_f32 v248, v248, v250\n"
        "v_cvt_pk_bf16_f32 v248, v248, v248\n"
        "v_lshlrev_b32 v248, 16, v248\n"
        "v_lshl_add_u32 v251, v0, 1, s98\n"
        "v_mul_f32 v248, v248, v249\n"
        "v_cvt_pk_bf16_f32 v248, v248, v248\n"
        "s_mov_b32 exec_hi, 0xffff\n"
        "ds_write_b16 v251, v248\n"
        "s_mov_b32 exec_hi, -1\n"
        "v_mov_b32 v254, 155656\n"
        "v_mov_b32 v255, 1\n"
        "s_mov_b64 exec, 1\n"
        "ds_add_u32 v254, v255\n"
        "s_mov_b64 exec, -1\n"
        "v_lshrrev_b32 v252, 2, v15\n"
        "v_and_b32 v250, 3, v0\n"
        "v_lshrrev_b32 v251, 1, v252\n"
        "v_and_b32 v253, 1, v14\n"
        "v_lshl_or_b32 v251, v253, 1, v251\n"
        "v_xor_b32 v251, s45, v251\n"
        "v_lshl_add_u32 v253, v14, 3, v252\n"
        "v_lshlrev_b32 v249, 7, v253\n"
        "v_lshl_add_u32 v249, v251, 5, v249\n"
        "v_lshl_add_u32 v249, v250, 3, v249\n"
        "v_lshlrev_b32 v26, 4, v14\n"
        "v_add_u32 v249, 41216, v249\n"
        "v_add_u32 v248, 162048, v26\n"
        "s_mov_b32 s95, 1048576\n"
        "s_nop 0\n"
        ".Lmk_c51:\n"
        "ds_read_b32 v255, v254\n"
        "s_waitcnt lgkmcnt(0)\n"
        "v_readfirstlane_b32 s94, v255\n"
        "s_nop 0\n"
        "s_cmp_ge_u32 s94, 4\n"
        "s_cbranch_scc1 .Lmk_d52\n"
        "s_sub_u32 s95, s95, 1\n"
        "s_cbranch_scc0 .Lmk_c51\n"
        ".Lmk_d52:\n"
        "ds_read_b128 v[104:107], v248 offset:0\n"
        "ds_read_b64_tr_b16 v[108:109], v249 offset:0\n"
        "ds_read_b64_tr_b16 v[110:111], v249 offset:512\n"
        "ds_read_b128 v[112:115], v248 offset:64\n"
        "ds_read_b64_tr_b16 v[116:117], v249 offset:4096\n"
        "ds_read_b64_tr_b16 v[118:119], v249 offset:4608\n"
        "ds_read_b128 v[120:123], v248 offset:128\n"
        "ds_read_b64_tr_b16 v[124:125], v249 offset:8192\n"
        "ds_read_b64_tr_b16 v[126:127], v249 offset:8704\n"
        "ds_read_b128 v[128:131], v248 offset:192\n"
        "ds_read_b64_tr_b16 v[132:133], v249 offset:12288\n"
        "ds_read_b64_tr_b16 v[134:135], v249 offset:12800\n"
        "ds_read_b128 v[136:139], v248 offset:256\n"
        "ds_read_b64_tr_b16 v[140:141], v249 offset:16384\n"
        "ds_read_b64_tr_b16 v[142:143], v249 offset:16896\n"
        "ds_read_b128 v[144:147], v248 offset:320\n"
        "ds_read_b64_tr_b16 v[148:149], v249 offset:20480\n"
        "ds_read_b64_tr_b16 v[150:151], v249 offset:20992\n"
        "s_mul_i32 s98, s47, 9\n"
        "s_add_u32 s98, s98, s48\n"
        "s_lshl2_add_u32 s98, s98, s45\n"
        "s_lshl_b32 s98, s98, 7\n"
        "s_add_u32 s98, s98, 327680\n"
        "v_mov_b32 v254, s40\n"
        "v_mov_b32 v255, s41\n"
        "s_waitcnt lgkmcnt(15)\n"
        "v_mfma_f32_16x16x32_bf16 v[16:19], v[108:111], v[104:107], 0\n"
        "s_waitcnt lgkmcnt(12)\n"
        "v_mfma_f32_16x16x32_bf16 v[16:19], v[116:119], v[112:115], v[16:19]\n"
        "s_waitcnt lgkmcnt(9)\n"
        "v_mfma_f32_16x16x32_bf16 v[16:19], v[124:127], v[120:123], v[16:19]\n"
        "s_waitcnt lgkmcnt(6)\n"
        "v_mfma_f32_16x16x32_bf16 v[16:19], v[132:135], v[128:131], v[16:19]\n"
        "s_waitcnt lgkmcnt(3)\n"
        "v_mfma_f32_16x16x32_bf16 v[16:19], v[140:143], v[136:139], v[16:19]\n"
        "s_waitcnt lgkmcnt(0)\n"
        "v_mfma_f32_16x16x32_bf16 v[16:19], v[148:151], v[144:147], v[16:19]\n"
        "s_nop 7\n"
        "v_cvt_pk_bf16_f32 v252, v16, v17\n"
        "v_cvt_pk_bf16_f32 v253, v18, v19\n"
        "s_mov_b32 exec_lo, 0x00010001\n"
        "s_mov_b32 exec_hi, 0x00010001\n"
        "buffer_store_dwordx4 v[252:255], v26, s[12:15], s98 offen sc1\n"
        "s_mov_b64 exec, -1\n"
        "s_branch .Lmk_s50\n"
        ".Lmk_n49:\n"
        ".Lmk_s50:\n"
        "s_setprio 0\n"
        "v_mov_b32 v254, 155688\n"
        "v_mov_b32 v255, 1\n"
        "s_mov_b64 exec, 1\n"
        "ds_add_u32 v254, v255\n"
        "s_mov_b64 exec, -1\n"
        ".Lmk_g147:\n"
        "v_lshrrev_b32 v219, 1, v14\n"
        "s_and_b32 s98, s45, 1\n"
        "s_lshl2_add_u32 s99, s63, s98\n"
        "v_lshrrev_b32 v212, 1, v15\n"
        "v_and_b32 v213, 1, v15\n"
        "v_lshl_add_u32 v212, v213, 4, v212\n"
        "v_lshl_add_u32 v1, s99, 3, v212\n"
        "v_mul_u32_u24 v212, 1152, v1\n"
        "v_lshl_add_u32 v13, v14, 3, v212\n"
        "s_lshl_b32 s92, s3, 6\n"
        "v_and_b32 v213, 1, v14\n"
        "v_lshl_add_u32 v212, v213, 5, v212\n"
        "v_lshl_add_u32 v212, v219, 4, v212\n"
        "v_add_u32 v12, s92, v212\n"
        "v_or_b32 v212, s3, v219\n"
        "v_lshl_or_b32 v13, v212, 31, v13\n"
        "buffer_load_dwordx4 v[2:5], v12, s[4:7], s97 offen\n"
        "buffer_load_dwordx2 v[6:7], v13, s[4:7], s97 offen offset:128\n"
        "v_lshlrev_b32 v214, 3, v1\n"
        "s_mov_b32 s93, 16384\n"
        "buffer_load_dwordx2 v[8:9], v214, s[12:15], s93 offen sc1\n"
        "s_lshl1_add_u32 s99, s63, s98\n"
        "v_lshl_add_u32 v212, s99, 4, v15\n"
        "v_lshlrev_b32 v213, 15, v14\n"
        "v_lshl_add_u32 v11, v212, 2, v213\n"
        "v_lshl_add_u32 v10, v212, 4, v213\n"
        "s_lshl_b32 s99, s63, 9\n"
        "s_add_u32 s99, s99, 655360\n"
        "v_and_b32 v212, 24, v0\n"
        "v_add_u32 v212, v212, v0\n"
        "v_lshl_add_u32 v253, v212, 3, s99\n"
        "v_lshrrev_b32 v216, 2, v15\n"
        "v_and_b32 v215, 3, v0\n"
        "v_and_b32 v217, 1, v216\n"
        "v_lshl_add_u32 v215, v217, 1, v215\n"
        "v_lshlrev_b32 v215, 3, v215\n"
        "v_add_u32 v215, 72704, v215\n"
        "v_mul_u32_u24 v217, 144, v217\n"
        "v_and_b32 v218, 2, v0\n"
        "v_lshl_add_u32 v217, v218, 3, v217\n"
        "v_mul_u32_u24 v212, 20, v219\n"
        "v_and_b32 v213, 1, v14\n"
        "v_lshl_add_u32 v212, v213, 2, v212\n"
        "v_add_u32 v212, v212, v216\n"
        "s_mul_i32 s99, s3, 288\n"
        "v_add_u32 v212, s99, v212\n"
        "v_mul_u32_u24 v213, 72, v14\n"
        "s_add_u32 s99, s99, 16\n"
        "v_add3_u32 v213, v213, v216, s99\n"
        "s_movk_i32 s99, 144\n"
        "v_mad_u32_u24 v195, v212, s99, v215\n"
        "v_mad_u32_u24 v201, v213, s99, v215\n"
        "v_add_u32 v193, s92, v195\n"
        "v_subrev_u32 v194, s92, v195\n"
        "v_sub_u32 v195, v195, v217\n"
        "v_add_u32 v199, s92, v201\n"
        "v_subrev_u32 v200, s92, v201\n"
        "v_sub_u32 v201, v201, v217\n"
        "s_mul_i32 s99, s42, 3072\n"
        "v_lshl_add_u32 v252, v0, 4, s99\n"
        "s_cmp_eq_u32 s46, 1\n"
        "s_cbranch_scc0 .Lmk_s53\n"
        "s_waitcnt vmcnt(3)\n"
        "v_mov_b32 v248, s40\n"
        "v_mov_b32 v249, s41\n"
        "s_mov_b64 exec, 1\n"
        "buffer_store_dwordx2 v[248:249], off, s[12:15], s56 sc1\n"
        "s_mov_b64 exec, -1\n"
        ".Lmk_s53:\n"
        "v_mov_b32 v254, 155688\n"
        "s_mov_b32 s95, 1048576\n"
        "s_nop 0\n"
        ".Lmk_c54:\n"
        "ds_read_b32 v255, v254\n"
        "s_waitcnt lgkmcnt(0)\n"
        "v_readfirstlane_b32 s94, v255\n"
        "s_nop 0\n"
        "s_cmp_ge_u32 s94, 8\n"
        "s_cbranch_scc1 .Lmk_d55\n"
        "s_sub_u32 s95, s95, 1\n"
        "s_cbranch_scc0 .Lmk_c54\n"
        ".Lmk_d55:\n"
        "ds_read_b32 v255, v254 offset:8\n"
        "ds_read_b64_tr_b16 v[116:117], v193 offset:36288\n"
        "ds_read_b64_tr_b16 v[118:119], v193 offset:37440\n"
        "ds_read_b64_tr_b16 v[120:121], v193 offset:36320\n"
        "ds_read_b64_tr_b16 v[122:123], v193 offset:37472\n"
        "ds_read_b64_tr_b16 v[124:125], v194 offset:36352\n"
        "ds_read_b64_tr_b16 v[126:127], v194 offset:37504\n"
        "ds_read_b64_tr_b16 v[128:129], v194 offset:36384\n"
        "ds_read_b64_tr_b16 v[130:131], v194 offset:37536\n"
        "ds_read_b64_tr_b16 v[132:133], v195 offset:36416\n"
        "ds_read_b64_tr_b16 v[134:135], v195 offset:37568\n"
        "ds_read_b64_tr_b16 v[136:137], v199 offset:0\n"
        "ds_read_b64_tr_b16 v[138:139], v199 offset:5184\n"
        "ds_read_b64_tr_b16 v[140:141], v199 offset:32\n"
        "ds_read_b64_tr_b16 v[142:143], v199 offset:5216\n"
        "ds_read_b64_tr_b16 v[144:145], v200 offset:64\n"
        "ds_read_b64_tr_b16 v[146:147], v200 offset:5248\n"
        "ds_read_b64_tr_b16 v[148:149], v200 offset:96\n"
        "ds_read_b64_tr_b16 v[150:151], v200 offset:5280\n"
        "ds_read_b64_tr_b16 v[152:153], v201 offset:128\n"
        "ds_read_b64_tr_b16 v[154:155], v201 offset:5312\n"
        "ds_read_b64_tr_b16 v[16:17], v193 offset:0\n"
        "ds_read_b64_tr_b16 v[18:19], v193 offset:1152\n"
        "ds_read_b64_tr_b16 v[20:21], v193 offset:32\n"
        "ds_read_b64_tr_b16 v[22:23], v193 offset:1184\n"
        "ds_read_b64_tr_b16 v[24:25], v194 offset:64\n"
        "ds_read_b64_tr_b16 v[26:27], v194 offset:1216\n"
        "ds_read_b64_tr_b16 v[28:29], v194 offset:96\n"
        "ds_read_b64_tr_b16 v[30:31], v194 offset:1248\n"
        "ds_read_b64_tr_b16 v[32:33], v195 offset:128\n"
        "ds_read_b64_tr_b16 v[34:35], v195 offset:1280\n"
        "ds_read_b64_tr_b16 v[36:37], v193 offset:5184\n"
        "ds_read_b64_tr_b16 v[38:39], v193 offset:6336\n"
        "ds_read_b64_tr_b16 v[40:41], v193 offset:5216\n"
        "ds_read_b64_tr_b16 v[42:43], v193 offset:6368\n"
        "ds_read_b64_tr_b16 v[44:45], v194 offset:5248\n"
        "ds_read_b64_tr_b16 v[46:47], v194 offset:6400\n"
        "ds_read_b64_tr_b16 v[48:49], v194 offset:5280\n"
        "ds_read_b64_tr_b16 v[50:51], v194 offset:6432\n"
        "ds_read_b64_tr_b16 v[52:53], v195 offset:5312\n"
        "ds_read_b64_tr_b16 v[54:55], v195 offset:6464\n"
        "ds_read_b64_tr_b16 v[56:57], v193 offset:10368\n"
        "ds_read_b64_tr_b16 v[58:59], v193 offset:11520\n"
        "ds_read_b64_tr_b16 v[60:61], v193 offset:10400\n"
        "ds_read_b64_tr_b16 v[62:63], v193 offset:11552\n"
        "ds_read_b64_tr_b16 v[64:65], v194 offset:10432\n"
        "ds_read_b64_tr_b16 v[66:67], v194 offset:11584\n"
        "ds_read_b64_tr_b16 v[68:69], v194 offset:10464\n"
        "ds_read_b64_tr_b16 v[70:71], v194 offset:11616\n"
        "ds_read_b64_tr_b16 v[72:73], v195 offset:10496\n"
        "ds_read_b64_tr_b16 v[74:75], v195 offset:11648\n"
        "ds_read_b64_tr_b16 v[76:77], v193 offset:15552\n"
        "ds_read_b64_tr_b16 v[78:79], v193 offset:16704\n"
        "ds_read_b64_tr_b16 v[80:81], v193 offset:15584\n"
        "ds_read_b64_tr_b16 v[82:83], v193 offset:16736\n"
        "ds_read_b64_tr_b16 v[84:85], v194 offset:15616\n"
        "ds_read_b64_tr_b16 v[86:87], v194 offset:16768\n"
        "ds_read_b64_tr_b16 v[88:89], v194 offset:15648\n"
        "ds_read_b64_tr_b16 v[90:91], v194 offset:16800\n"
        "ds_read_b64_tr_b16 v[92:93], v195 offset:15680\n"
        "ds_read_b64_tr_b16 v[94:95], v195 offset:16832\n"
        "s_cmp_eq_u32 s45, 0\n"
        "s_cbranch_scc0 .Lmk_s56\n"
        "s_mov_b32 exec_hi, 0\n"
        "buffer_load_dwordx2 v[246:247], v253, s[12:15], 0 offen sc1\n"
        "s_mov_b32 exec_hi, -1\n"
        ".Lmk_s56:\n"
        "ds_read_b64_tr_b16 v[96:97], v193 offset:20736\n"
        "ds_read_b64_tr_b16 v[98:99], v193 offset:21888\n"
        "ds_read_b64_tr_b16 v[100:101], v193 offset:20768\n"
        "ds_read_b64_tr_b16 v[102:103], v193 offset:21920\n"
        "ds_read_b64_tr_b16 v[104:105], v194 offset:20800\n"
        "ds_read_b64_tr_b16 v[106:107], v194 offset:21952\n"
        "ds_read_b64_tr_b16 v[108:109], v194 offset:20832\n"
        "ds_read_b64_tr_b16 v[110:111], v194 offset:21984\n"
        "ds_read_b64_tr_b16 v[112:113], v195 offset:20864\n"
        "ds_read_b64_tr_b16 v[114:115], v195 offset:22016\n"
        ".Lmk_f0:\n"
        "s_waitcnt lgkmcnt(15)\n"
        "v_readfirstlane_b32 s58, v255\n"
        "s_nop 0\n"
        "s_andn2_b32 s57, 0x7ff, s58\n"
        "s_and_b32 s98, s45, 1\n"
        "s_lshl2_add_u32 s98, s63, s98\n"
        "s_lshr_b32 s99, s57, 3\n"
        "s_andn2_b32 s99, s99, 2\n"
        "s_cmp_eq_u32 s98, s99\n"
        "s_cselect_b32 s96, 1, 0\n"
        "s_cmp_eq_u32 s96, 1\n"
        "s_cbranch_scc0 .Lmk_s57\n"
        "s_andn2_b32 s98, s58, 0x7ff\n"
        "s_not_b32 s99, s98\n"
        "s_andn2_b32 s99, s99, 0x7ff\n"
        "s_and_b32 s92, s98, 0x7fffffff\n"
        "s_cmp_lt_i32 s98, 0\n"
        "s_cselect_b32 s98, s92, s99\n"
        "v_mov_b32 v248, s98\n"
        "v_and_b32 v250, 1, v15\n"
        "v_lshlrev_b32 v250, 4, v250\n"
        "v_cvt_pk_bf16_f32 v248, v248, v248\n"
        "v_lshlrev_b32 v248, 16, v248\n"
        "v_mul_f32 v249, 0xbfb8aa3b, v248\n"
        "v_exp_f32 v249, v249\n"
        "s_nop 0\n"
        "v_add_f32 v249, 1.0, v249\n"
        "v_rcp_f32 v249, v249\n"
        "s_nop 0\n"
        "v_cvt_pk_bf16_f32 v249, v249, v249\n"
        "v_lshlrev_b32 v249, 16, v249\n"
        "v_lshl_add_u32 v251, s3, 3, v15\n"
        "v_and_or_b32 v248, v15, 1, 16\n"
        "v_cmp_gt_u32 vcc, 8, v15\n"
        "s_lshr_b32 s98, s97, 3\n"
        "v_readfirstlane_b32 s53, v249\n"
        "v_cndmask_b32_e32 v251, v248, v251, vcc\n"
        "v_add_u32 v251, s98, v251\n"
        "v_lshrrev_b32 v248, 2, v251\n"
        "v_mul_u32_u24 v248, 64, v248\n"
        "v_lshl_add_u32 v251, v251, 4, v248\n"
        "s_movk_i32 s98, 4608\n"
        "v_mad_u32_u24 v192, v14, s98, v251\n"
        "v_mov_b32 v248, 0x3f80\n"
        "v_lshlrev_b32 v248, v250, v248\n"
        "v_and_b32 v251, 2, v15\n"
        "v_cmp_eq_u32 vcc, 0, v251\n"
        "v_lshlrev_b32 v190, 2, v14\n"
        "v_and_b32 v191, 4, v190\n"
        "v_cndmask_b32_e32 v188, 0, v248, vcc\n"
        "v_xor_b32 v189, v188, v248\n"
        ".Lmk_s57:\n"
        "s_waitcnt lgkmcnt(0)\n"
        "s_lshl_b32 s98, s3, 20\n"
        "s_add_u32 s98, s98, 9437184\n"
        "s_waitcnt vmcnt(0)\n"
        "s_mul_i32 s99, s46, 24\n"
        "s_add_u32 s99, s99, 155660\n"
        "s_cmp_eq_u32 s45, 0\n"
        "s_cbranch_scc0 .Lmk_s58\n"
        "v_cmp_ne_u64 vcc, s[40:41], v[246:247]\n"
        "s_nop 0\n"
        "s_cmp_eq_u32 vcc_lo, 0\n"
        "s_cbranch_scc1 .Lmk_e59\n"
        "s_mov_b32 exec_hi, 0\n"
        "s_mov_b32 s95, 65536\n"
        ".Lmk_p60:\n"
        "buffer_load_dwordx2 v[248:249], v253, s[12:15], 0 offen sc1\n"
        "s_waitcnt vmcnt(0)\n"
        "v_cmp_ne_u64 vcc, s[40:41], v[248:249]\n"
        "s_nop 0\n"
        "s_cmp_eq_u64 vcc, 0\n"
        "s_cbranch_scc1 .Lmk_d61\n"
        "s_sleep 1\n"
        "s_sub_u32 s95, s95, 1\n"
        "s_cbranch_scc0 .Lmk_p60\n"
        ".Lmk_d61:\n"
        "s_mov_b64 exec, -1\n"
        ".Lmk_e59:\n"
        "v_mov_b32 v254, s99\n"
        "v_mov_b32 v255, 1\n"
        "s_mov_b64 exec, 1\n"
        "ds_add_u32 v254, v255\n"
        "s_mov_b64 exec, -1\n"
        ".Lmk_s58:\n"
        "v_mov_b32 v254, s99\n"
        "s_mov_b32 s95, 1048576\n"
        "s_nop 0\n"
        ".Lmk_c62:\n"
        "ds_read_b32 v255, v254\n"
        "s_waitcnt lgkmcnt(0)\n"
        "v_readfirstlane_b32 s94, v255\n"
        "s_nop 0\n"
        "s_cmp_ge_u32 s94, 1\n"
        "s_cbranch_scc1 .Lmk_d63\n"
        "s_sub_u32 s95, s95, 1\n"
        "s_cbranch_scc0 .Lmk_c62\n"
        ".Lmk_d63:\n"
        "s_lshl_b32 s99, s3, 17\n"
        "s_add_u32 s99, s99, 11534336\n"
        "buffer_load_dwordx4 v[196:199], v10, s[12:15], s98 offen sc1\n"
        "s_add_u32 s98, s98, 131072\n"
        "buffer_load_dwordx4 v[200:203], v10, s[12:15], s98 offen sc1\n"
        "s_add_u32 s98, s98, 131072\n"
        "buffer_load_dwordx4 v[204:207], v10, s[12:15], s98 offen sc1\n"
        "s_add_u32 s98, s98, 131072\n"
        "buffer_load_dwordx4 v[208:211], v10, s[12:15], s98 offen sc1\n"
        "s_add_u32 s98, s98, 131072\n"
        "buffer_load_dwordx4 v[212:215], v10, s[12:15], s98 offen sc1\n"
        "s_add_u32 s98, s98, 131072\n"
        "buffer_load_dwordx4 v[216:219], v10, s[12:15], s98 offen sc1\n"
        "s_add_u32 s98, s98, 131072\n"
        "buffer_load_dwordx4 v[220:223], v10, s[12:15], s98 offen sc1\n"
        "s_add_u32 s98, s98, 131072\n"
        "buffer_load_dwordx4 v[224:227], v10, s[12:15], s98 offen sc1\n"
        "buffer_load_dword v228, v11, s[12:15], s99 offen sc1\n"
        "s_add_u32 s99, s99, 8192\n"
        "buffer_load_dword v229, v11, s[12:15], s99 offen sc1\n"
        "s_add_u32 s99, s99, 8192\n"
        "buffer_load_dword v230, v11, s[12:15], s99 offen sc1\n"
        "s_add_u32 s99, s99, 8192\n"
        "buffer_load_dword v231, v11, s[12:15], s99 offen sc1\n"
        "s_cmp_eq_u32 s96, 1\n"
        "s_cbranch_scc0 .Lmk_s64\n"
        "s_mov_b32 s98, 327680\n"
        "buffer_load_dwordx4 v[156:159], v192, s[12:15], s98 offen sc1\n"
        "s_mov_b32 s98, 346112\n"
        "buffer_load_dwordx4 v[160:163], v192, s[12:15], s98 offen sc1\n"
        "s_mov_b32 s98, 364544\n"
        "buffer_load_dwordx4 v[164:167], v192, s[12:15], s98 offen sc1\n"
        "s_mov_b32 s98, 382976\n"
        "buffer_load_dwordx4 v[168:171], v192, s[12:15], s98 offen sc1\n"
        "s_mov_b32 s98, 401408\n"
        "buffer_load_dwordx4 v[172:175], v192, s[12:15], s98 offen sc1\n"
        "s_mov_b32 s98, 419840\n"
        "buffer_load_dwordx4 v[176:179], v192, s[12:15], s98 offen sc1\n"
        ".Lmk_s64:\n"
        "v_mov_b32 v254, 155692\n"
        "s_mov_b32 s95, 1048576\n"
        "s_nop 0\n"
        ".Lmk_c65:\n"
        "ds_read_b32 v255, v254\n"
        "s_waitcnt lgkmcnt(0)\n"
        "v_readfirstlane_b32 s94, v255\n"
        "s_nop 0\n"
        "s_cmp_ge_u32 s94, 8\n"
        "s_cbranch_scc1 .Lmk_d66\n"
        "s_sub_u32 s95, s95, 1\n"
        "s_cbranch_scc0 .Lmk_c65\n"
        ".Lmk_d66:\n"
        "v_cmp_ne_u32 vcc, s40, v9\n"
        "s_nop 0\n"
        "s_cmp_eq_u64 vcc, 0\n"
        "s_cbranch_scc0 .Lmk_gspin\n"
        ".Lmk_gok:\n"
        "v_sub_f32 v9, 1.0, v8\n"
        "v_permlane16_swap_b32 v2, v4\n"
        "v_permlane16_swap_b32 v3, v5\n"
        "s_waitcnt vmcnt(11) lgkmcnt(0)\n"
        "v_mfma_f32_16x16x32_bf16 v[232:235], v[16:19], v[196:199], 0\n"
        "v_mfma_f32_16x16x32_bf16 v[236:239], v[20:23], v[196:199], 0\n"
        "v_mfma_f32_16x16x32_bf16 v[240:243], v[24:27], v[196:199], 0\n"
        "v_mfma_f32_16x16x32_bf16 v[244:247], v[28:31], v[196:199], 0\n"
        "v_mfma_f32_16x16x32_bf16 v[248:251], v[32:35], v[196:199], 0\n"
        "s_waitcnt vmcnt(10)\n"
        "v_mfma_f32_16x16x32_bf16 v[232:235], v[36:39], v[200:203], v[232:235]\n"
        "ds_read_b64_tr_b16 v[36:37], v193 offset:25920\n"
        "ds_read_b64_tr_b16 v[38:39], v193 offset:27072\n"
        "v_lshlrev_b32 v16, 16, v2\n"
        "v_mfma_f32_16x16x32_bf16 v[236:239], v[40:43], v[200:203], v[236:239]\n"
        "ds_read_b64_tr_b16 v[40:41], v193 offset:25952\n"
        "ds_read_b64_tr_b16 v[42:43], v193 offset:27104\n"
        "v_and_b32 v17, 0xffff0000, v2\n"
        "v_mfma_f32_16x16x32_bf16 v[240:243], v[44:47], v[200:203], v[240:243]\n"
        "ds_read_b64_tr_b16 v[44:45], v194 offset:25984\n"
        "ds_read_b64_tr_b16 v[46:47], v194 offset:27136\n"
        "v_lshlrev_b32 v18, 16, v3\n"
        "v_mfma_f32_16x16x32_bf16 v[244:247], v[48:51], v[200:203], v[244:247]\n"
        "ds_read_b64_tr_b16 v[48:49], v194 offset:26016\n"
        "ds_read_b64_tr_b16 v[50:51], v194 offset:27168\n"
        "v_and_b32 v19, 0xffff0000, v3\n"
        "v_mfma_f32_16x16x32_bf16 v[248:251], v[52:55], v[200:203], v[248:251]\n"
        "ds_read_b64_tr_b16 v[52:53], v195 offset:26048\n"
        "ds_read_b64_tr_b16 v[54:55], v195 offset:27200\n"
        "v_lshlrev_b32 v20, 16, v4\n"
        "s_waitcnt vmcnt(9)\n"
        "v_mfma_f32_16x16x32_bf16 v[232:235], v[56:59], v[204:207], v[232:235]\n"
        "ds_read_b64_tr_b16 v[56:57], v193 offset:31104\n"
        "ds_read_b64_tr_b16 v[58:59], v193 offset:32256\n"
        "v_and_b32 v21, 0xffff0000, v4\n"
        "v_mfma_f32_16x16x32_bf16 v[236:239], v[60:63], v[204:207], v[236:239]\n"
        "ds_read_b64_tr_b16 v[60:61], v193 offset:31136\n"
        "ds_read_b64_tr_b16 v[62:63], v193 offset:32288\n"
        "v_lshlrev_b32 v22, 16, v5\n"
        "v_mfma_f32_16x16x32_bf16 v[240:243], v[64:67], v[204:207], v[240:243]\n"
        "ds_read_b64_tr_b16 v[64:65], v194 offset:31168\n"
        "ds_read_b64_tr_b16 v[66:67], v194 offset:32320\n"
        "v_and_b32 v23, 0xffff0000, v5\n"
        "v_mfma_f32_16x16x32_bf16 v[244:247], v[68:71], v[204:207], v[244:247]\n"
        "ds_read_b64_tr_b16 v[68:69], v194 offset:31200\n"
        "ds_read_b64_tr_b16 v[70:71], v194 offset:32352\n"
        "v_lshlrev_b32 v24, 16, v6\n"
        "v_mfma_f32_16x16x32_bf16 v[248:251], v[72:75], v[204:207], v[248:251]\n"
        "ds_read_b64_tr_b16 v[72:73], v195 offset:31232\n"
        "ds_read_b64_tr_b16 v[74:75], v195 offset:32384\n"
        "v_and_b32 v25, 0xffff0000, v6\n"
        "s_waitcnt vmcnt(8)\n"
        "v_mfma_f32_16x16x32_bf16 v[232:235], v[76:79], v[208:211], v[232:235]\n"
        "v_lshlrev_b32 v26, 16, v7\n"
        "v_mfma_f32_16x16x32_bf16 v[236:239], v[80:83], v[208:211], v[236:239]\n"
        "v_and_b32 v27, 0xffff0000, v7\n"
        "v_mfma_f32_16x16x32_bf16 v[240:243], v[84:87], v[208:211], v[240:243]\n"
        "v_mul_f32 v16, v16, v9\n"
        "v_mfma_f32_16x16x32_bf16 v[244:247], v[88:91], v[208:211], v[244:247]\n"
        "v_mul_f32 v17, v17, v9\n"
        "v_mfma_f32_16x16x32_bf16 v[248:251], v[92:95], v[208:211], v[248:251]\n"
        "v_mul_f32 v18, v18, v9\n"
        "s_waitcnt vmcnt(7)\n"
        "v_mfma_f32_16x16x32_bf16 v[232:235], v[96:99], v[212:215], v[232:235]\n"
        "v_mul_f32 v19, v19, v9\n"
        "v_mfma_f32_16x16x32_bf16 v[236:239], v[100:103], v[212:215], v[236:239]\n"
        "v_mul_f32 v20, v20, v9\n"
        "v_mfma_f32_16x16x32_bf16 v[240:243], v[104:107], v[212:215], v[240:243]\n"
        "v_mul_f32 v21, v21, v9\n"
        "v_mfma_f32_16x16x32_bf16 v[244:247], v[108:111], v[212:215], v[244:247]\n"
        "v_mul_f32 v22, v22, v9\n"
        "v_mfma_f32_16x16x32_bf16 v[248:251], v[112:115], v[212:215], v[248:251]\n"
        "v_mul_f32 v23, v23, v9\n"
        "s_waitcnt vmcnt(6) lgkmcnt(10)\n"
        "v_mfma_f32_16x16x32_bf16 v[232:235], v[36:39], v[216:219], v[232:235]\n"
        "v_mul_f32 v24, v24, v9\n"
        "v_mfma_f32_16x16x32_bf16 v[236:239], v[40:43], v[216:219], v[236:239]\n"
        "v_mul_f32 v25, v25, v9\n"
        "v_mfma_f32_16x16x32_bf16 v[240:243], v[44:47], v[216:219], v[240:243]\n"
        "v_mul_f32 v26, v26, v9\n"
        "v_mfma_f32_16x16x32_bf16 v[244:247], v[48:51], v[216:219], v[244:247]\n"
        "v_mul_f32 v27, v27, v9\n"
        "v_mfma_f32_16x16x32_bf16 v[248:251], v[52:55], v[216:219], v[248:251]\n"
        "s_waitcnt vmcnt(5) lgkmcnt(0)\n"
        "v_mfma_f32_16x16x32_bf16 v[232:235], v[56:59], v[220:223], v[232:235]\n"
        "v_mfma_f32_16x16x32_bf16 v[236:239], v[60:63], v[220:223], v[236:239]\n"
        "v_mfma_f32_16x16x32_bf16 v[240:243], v[64:67], v[220:223], v[240:243]\n"
        "v_mfma_f32_16x16x32_bf16 v[244:247], v[68:71], v[220:223], v[244:247]\n"
        "v_mfma_f32_16x16x32_bf16 v[248:251], v[72:75], v[220:223], v[248:251]\n"
        "s_waitcnt vmcnt(4)\n"
        "v_mfma_f32_16x16x32_bf16 v[232:235], v[116:119], v[224:227], v[232:235]\n"
        "v_mfma_f32_16x16x32_bf16 v[236:239], v[120:123], v[224:227], v[236:239]\n"
        "v_mfma_f32_16x16x32_bf16 v[240:243], v[124:127], v[224:227], v[240:243]\n"
        "v_mfma_f32_16x16x32_bf16 v[244:247], v[128:131], v[224:227], v[244:247]\n"
        "v_mfma_f32_16x16x32_bf16 v[248:251], v[132:135], v[224:227], v[248:251]\n"
        "s_waitcnt vmcnt(0)\n"
        "v_mfma_f32_16x16x32_bf16 v[232:235], v[136:139], v[228:231], v[232:235]\n"
        "v_mfma_f32_16x16x32_bf16 v[236:239], v[140:143], v[228:231], v[236:239]\n"
        "v_mfma_f32_16x16x32_bf16 v[240:243], v[144:147], v[228:231], v[240:243]\n"
        "v_mfma_f32_16x16x32_bf16 v[244:247], v[148:151], v[228:231], v[244:247]\n"
        "v_mfma_f32_16x16x32_bf16 v[248:251], v[152:155], v[228:231], v[248:251]\n"
        "s_nop 5\n"
        "ds_write_b128 v252, v[240:243] offset:0\n"
        "ds_write_b128 v252, v[244:247] offset:1024\n"
        "ds_write_b128 v252, v[248:251] offset:2048\n"
        "s_xor_b32 s99, s42, 2\n"
        "s_mul_i32 s99, s99, 3072\n"
        "v_lshl_add_u32 v252, v0, 4, s99\n"
        "s_cmp_eq_u32 s96, 1\n"
        "s_cbranch_scc0 .Lmk_s67\n"
        "s_mov_b32 s95, 65536\n"
        ".Lmk_yre:\n"
        "v_cmp_ne_u64 vcc, s[40:41], v[158:159]\n"
        "v_cmp_ne_u64 s[90:91], s[40:41], v[162:163]\n"
        "s_nop 0\n"
        "s_or_b64 vcc, vcc, s[90:91]\n"
        "v_cmp_ne_u64 s[90:91], s[40:41], v[166:167]\n"
        "s_nop 0\n"
        "s_or_b64 vcc, vcc, s[90:91]\n"
        "v_cmp_ne_u64 s[90:91], s[40:41], v[170:171]\n"
        "s_nop 0\n"
        "s_or_b64 vcc, vcc, s[90:91]\n"
        "v_cmp_ne_u64 s[90:91], s[40:41], v[174:175]\n"
        "s_nop 0\n"
        "s_or_b64 vcc, vcc, s[90:91]\n"
        "v_cmp_ne_u64 s[90:91], s[40:41], v[178:179]\n"
        "s_nop 0\n"
        "s_or_b64 vcc, vcc, s[90:91]\n"
        "s_cbranch_scc0 .Lmk_yok\n"
        "s_mov_b32 s98, 327680\n"
        "buffer_load_dwordx4 v[156:159], v192, s[12:15], s98 offen sc1\n"
        "s_mov_b32 s98, 346112\n"
        "buffer_load_dwordx4 v[160:163], v192, s[12:15], s98 offen sc1\n"
        "s_mov_b32 s98, 364544\n"
        "buffer_load_dwordx4 v[164:167], v192, s[12:15], s98 offen sc1\n"
        "s_mov_b32 s98, 382976\n"
        "buffer_load_dwordx4 v[168:171], v192, s[12:15], s98 offen sc1\n"
        "s_mov_b32 s98, 401408\n"
        "buffer_load_dwordx4 v[172:175], v192, s[12:15], s98 offen sc1\n"
        "s_mov_b32 s98, 419840\n"
        "buffer_load_dwordx4 v[176:179], v192, s[12:15], s98 offen sc1\n"
        "s_waitcnt vmcnt(0)\n"
        "s_sub_u32 s95, s95, 1\n"
        "s_cbranch_scc0 .Lmk_yre\n"
        ".Lmk_yok:\n"
        "v_mfma_f32_16x16x16_bf16 v[28:31], v[188:189], v[156:157], 0\n"
        "v_mfma_f32_16x16x16_bf16 v[28:31], v[188:189], v[160:161], v[28:31]\n"
        "v_mfma_f32_16x16x16_bf16 v[28:31], v[188:189], v[164:165], v[28:31]\n"
        "v_mfma_f32_16x16x16_bf16 v[28:31], v[188:189], v[168:169], v[28:31]\n"
        "v_mfma_f32_16x16x16_bf16 v[28:31], v[188:189], v[172:173], v[28:31]\n"
        "v_mfma_f32_16x16x16_bf16 v[28:31], v[188:189], v[176:177], v[28:31]\n"
        "v_mul_f32 v32, s53, v9\n"
        "v_cmp_eq_u32 s[90:91], s57, v1\n"
        "s_and_b32 s98, s44, 7\n"
        "s_or_b32 s98, s98, s3\n"
        "s_cmp_eq_u32 s98, 0\n"
        "s_cbranch_scc0 .Lmk_s68\n"
        "v_mov_b32 v34, s40\n"
        "v_mov_b32 v35, s41\n"
        "s_mov_b64 exec, 1\n"
        "buffer_store_dwordx2 v[34:35], off, s[12:15], 0 sc1\n"
        "s_mov_b64 exec, -1\n"
        ".Lmk_s68:\n"
        "s_nop 1\n"
        "v_cvt_pk_bf16_f32 v28, v28, v28\n"
        "v_lshlrev_b32 v28, 16, v28\n"
        "v_cvt_pk_bf16_f32 v29, v29, v29\n"
        "v_lshlrev_b32 v29, 16, v29\n"
        "v_cvt_pk_bf16_f32 v30, v30, v30\n"
        "v_lshlrev_b32 v30, 16, v30\n"
        "v_cvt_pk_bf16_f32 v31, v31, v31\n"
        "v_lshlrev_b32 v31, 16, v31\n"
        "ds_bpermute_b32 v76, v190, v28 offset:0\n"
        "ds_bpermute_b32 v77, v190, v29 offset:0\n"
        "ds_bpermute_b32 v78, v190, v30 offset:0\n"
        "ds_bpermute_b32 v79, v190, v31 offset:0\n"
        "ds_bpermute_b32 v80, v190, v28 offset:16\n"
        "ds_bpermute_b32 v81, v190, v29 offset:16\n"
        "ds_bpermute_b32 v82, v190, v30 offset:16\n"
        "ds_bpermute_b32 v83, v190, v31 offset:16\n"
        "ds_bpermute_b32 v84, v191, v28 offset:32\n"
        "ds_bpermute_b32 v85, v191, v29 offset:32\n"
        "ds_bpermute_b32 v86, v191, v30 offset:32\n"
        "ds_bpermute_b32 v87, v191, v31 offset:32\n"
        "s_mov_b64 exec, s[90:91]\n"
        "s_waitcnt lgkmcnt(8)\n"
        "v_fmac_f32 v16, v76, v32\n"
        "v_fmac_f32 v17, v77, v32\n"
        "v_fmac_f32 v18, v78, v32\n"
        "v_fmac_f32 v19, v79, v32\n"
        "s_waitcnt lgkmcnt(4)\n"
        "v_fmac_f32 v20, v80, v32\n"
        "v_fmac_f32 v21, v81, v32\n"
        "v_fmac_f32 v22, v82, v32\n"
        "v_fmac_f32 v23, v83, v32\n"
        "s_waitcnt lgkmcnt(0)\n"
        "v_fmac_f32 v24, v84, v32\n"
        "v_fmac_f32 v25, v85, v32\n"
        "v_fmac_f32 v26, v86, v32\n"
        "v_fmac_f32 v27, v87, v32\n"
        "s_mov_b64 exec, -1\n"
        ".Lmk_s67:\n"
        ".Lmk_f1:\n"
        "s_waitcnt lgkmcnt(0)\n"
        "s_barrier\n"
        "ds_read_b128 v[40:43], v252 offset:0\n"
        "ds_read_b128 v[44:47], v252 offset:1024\n"
        "ds_read_b128 v[48:51], v252 offset:2048\n"
        "s_waitcnt lgkmcnt(2)\n"
        "v_add_f32 v40, v40, v232\n"
        "v_add_f32 v41, v41, v233\n"
        "v_add_f32 v42, v42, v234\n"
        "v_add_f32 v43, v43, v235\n"
        "v_fma_f32 v40, v40, v8, v16\n"
        "v_fma_f32 v41, v41, v8, v17\n"
        "v_fma_f32 v42, v42, v8, v18\n"
        "v_fma_f32 v43, v43, v8, v19\n"
        "v_cvt_pk_bf16_f32 v56, v40, v41\n"
        "v_cvt_pk_bf16_f32 v57, v42, v43\n"
        "s_waitcnt lgkmcnt(1)\n"
        "v_add_f32 v44, v44, v236\n"
        "v_add_f32 v45, v45, v237\n"
        "v_add_f32 v46, v46, v238\n"
        "v_add_f32 v47, v47, v239\n"
        "v_fma_f32 v44, v44, v8, v20\n"
        "v_fma_f32 v45, v45, v8, v21\n"
        "v_fma_f32 v46, v46, v8, v22\n"
        "v_fma_f32 v47, v47, v8, v23\n"
        "v_cvt_pk_bf16_f32 v58, v44, v45\n"
        "v_cvt_pk_bf16_f32 v59, v46, v47\n"
        "s_waitcnt lgkmcnt(0)\n"
        "v_permlane16_swap_b32 v56, v58\n"
        "v_permlane16_swap_b32 v57, v59\n"
        "buffer_store_dwordx4 v[56:59], v12, s[32:35], s97 offen \n"
        "v_add_f32 v48, v48, v248\n"
        "v_add_f32 v49, v49, v249\n"
        "v_add_f32 v50, v50, v250\n"
        "v_add_f32 v51, v51, v251\n"
        "v_fma_f32 v48, v48, v8, v24\n"
        "v_fma_f32 v49, v49, v8, v25\n"
        "v_fma_f32 v50, v50, v8, v26\n"
        "v_fma_f32 v51, v51, v8, v27\n"
        "v_cvt_pk_bf16_f32 v60, v48, v49\n"
        "v_cvt_pk_bf16_f32 v61, v50, v51\n"
        "buffer_store_dwordx2 v[60:61], v13, s[32:35], s97 offen offset:128 \n"
        "s_endpgm\n"
        ".Lmk_rgspin:\n"
        "s_mov_b32 s95, 65536\n"
        ".Lmk_r69:\n"
        "s_sleep 1\n"
        "v_and_b32 v12, 31, v0\n"
        "v_and_b32 v13, 24, v0\n"
        "v_add_u32 v12, v12, v13\n"
        "s_and_b32 s93, s2, 15\n"
        "s_mul_i32 s93, s93, 4352\n"
        "v_lshlrev_b32 v12, 3, v12\n"
        "s_add_u32 s93, s93, 12320768\n"
        "buffer_load_dwordx2 v[12:13], v12, s[12:15], s93 offen sc1\n"
        "s_waitcnt vmcnt(0)\n"
        "v_cmp_ne_u32 vcc, s40, v13\n"
        "s_nop 0\n"
        "s_cmp_eq_u64 vcc, 0\n"
        "s_nop 2\n"
        "s_cbranch_scc1 .Lmk_rgok\n"
        "s_sub_u32 s95, s95, 1\n"
        "s_cbranch_scc0 .Lmk_r69\n"
        "s_branch .Lmk_rgok\n"
        ".Lmk_wfspin:\n"
        "s_mov_b32 s95, 65536\n"
        ".Lmk_r70:\n"
        "s_sleep 1\n"
        "v_and_b32 v12, 31, v0\n"
        "v_and_b32 v13, 24, v0\n"
        "v_add_u32 v12, v12, v13\n"
        "s_and_b32 s93, s2, 15\n"
        "s_mul_i32 s93, s93, 4352\n"
        "v_lshlrev_b32 v12, 3, v12\n"
        "s_add_u32 s93, s93, 12321280\n"
        "buffer_load_dwordx2 v[12:13], v12, s[12:15], s93 offen sc1\n"
        "s_waitcnt vmcnt(0)\n"
        "v_cmp_ne_u32 vcc, s40, v13\n"
        "s_nop 0\n"
        "s_cmp_eq_u64 vcc, 0\n"
        "s_nop 2\n"
        "s_cbranch_scc1 .Lmk_wfok\n"
        "s_sub_u32 s95, s95, 1\n"
        "s_cbranch_scc0 .Lmk_r70\n"
        "s_branch .Lmk_wfok\n"
        ".Lmk_gspin:\n"
        "s_mov_b32 s95, 65536\n"
        ".Lmk_r71:\n"
        "s_sleep 1\n"
        "v_lshlrev_b32 v8, 3, v1\n"
        "s_mov_b32 s93, 16384\n"
        "buffer_load_dwordx2 v[8:9], v8, s[12:15], s93 offen sc1\n"
        "s_waitcnt vmcnt(0)\n"
        "v_cmp_ne_u32 vcc, s40, v9\n"
        "s_nop 0\n"
        "s_cmp_eq_u64 vcc, 0\n"
        "s_nop 2\n"
        "s_cbranch_scc1 .Lmk_gok\n"
        "s_sub_u32 s95, s95, 1\n"
        "s_cbranch_scc0 .Lmk_r71\n"
        "s_branch .Lmk_gok\n"
        ".Lmk_slow:\n"
        "v_mov_b32 v248, 0xff7fffff\n"
        "s_mov_b32 s93, 0\n"
        "v_mov_b32 v168, 0\n"
        "v_mov_b32 v169, 0\n"
        "v_mov_b32 v170, 0\n"
        "v_mov_b32 v171, 0\n"
        "v_mov_b32 v172, 0\n"
        "v_mov_b32 v173, 0\n"
        "v_mov_b32 v174, 0\n"
        "v_mov_b32 v175, 0\n"
        "v_mov_b32 v176, 0\n"
        "v_mov_b32 v177, 0\n"
        "v_mov_b32 v178, 0\n"
        "v_mov_b32 v179, 0\n"
        "v_mov_b32 v180, 0\n"
        "v_mov_b32 v181, 0\n"
        "v_mov_b32 v182, 0\n"
        "v_mov_b32 v183, 0\n"
        "s_nop 1\n"
        ".Lmk_sp:\n"
        "s_sub_i32 s92, 8, s100\n"
        "s_max_i32 s92, s92, 0\n"
        ".Lmk_sl:\n"
        "s_lshl_b32 s94, s92, 8\n"
        "v_add_u32 v249, s94, v230\n"
        "v_add_u32 v250, s94, v232\n"
        "v_add_u32 v251, s94, v233\n"
        "ds_read2st64_b64 v[192:195], v249 offset0:0 offset1:8\n"
        "ds_read_b64 v[200:201], v250\n"
        "ds_read_b64_tr_b16 v[208:209], v251 offset:0\n"
        "ds_read_b64_tr_b16 v[210:211], v251 offset:64\n"
        "ds_read_b64_tr_b16 v[212:213], v251 offset:128\n"
        "ds_read_b64_tr_b16 v[214:215], v251 offset:192\n"
        "s_cmp_eq_u32 s92, 0\n"
        "s_cselect_b32 s94, 1.0, 0\n"
        "s_cmp_eq_u32 s92, 8\n"
        "s_cselect_b32 s95, 1.0, 0\n"
        "v_mul_f32 v152, s94, v104\n"
        "v_fmac_f32 v152, s95, v120\n"
        "v_mul_f32 v153, s94, v105\n"
        "v_fmac_f32 v153, s95, v121\n"
        "v_mul_f32 v154, s94, v106\n"
        "v_fmac_f32 v154, s95, v122\n"
        "v_mul_f32 v155, s94, v107\n"
        "v_fmac_f32 v155, s95, v123\n"
        "v_mul_f32 v156, s94, v108\n"
        "v_fmac_f32 v156, s95, v124\n"
        "v_mul_f32 v157, s94, v109\n"
        "v_fmac_f32 v157, s95, v125\n"
        "v_mul_f32 v158, s94, v110\n"
        "v_fmac_f32 v158, s95, v126\n"
        "v_mul_f32 v159, s94, v111\n"
        "v_fmac_f32 v159, s95, v127\n"
        "v_mul_f32 v160, s94, v112\n"
        "v_fmac_f32 v160, s95, v128\n"
        "v_mul_f32 v161, s94, v113\n"
        "v_fmac_f32 v161, s95, v129\n"
        "v_mul_f32 v162, s94, v114\n"
        "v_fmac_f32 v162, s95, v130\n"
        "v_mul_f32 v163, s94, v115\n"
        "v_fmac_f32 v163, s95, v131\n"
        "v_mul_f32 v164, s94, v116\n"
        "v_fmac_f32 v164, s95, v132\n"
        "v_mul_f32 v165, s94, v117\n"
        "v_fmac_f32 v165, s95, v133\n"
        "v_mul_f32 v166, s94, v118\n"
        "v_fmac_f32 v166, s95, v134\n"
        "v_mul_f32 v167, s94, v119\n"
        "v_fmac_f32 v167, s95, v135\n"
        "s_waitcnt lgkmcnt(0)\n"
        "s_nop 0\n"
        "v_mfma_f32_32x32x16_bf16 v[136:151], v[192:195], v[224:227], v[152:167]\n"
        "v_mfma_f32_32x32x8_bf16 v[136:151], v[200:201], v[228:229], v[136:151]\n"
        "s_cmp_eq_u32 s93, 0\n"
        "s_nop 10\n"
        "s_cbranch_scc0 .Lmk_slow2\n"
        "v_max3_f32 v248, v136, v137, v248\n"
        "v_max3_f32 v248, v138, v139, v248\n"
        "v_max3_f32 v248, v140, v141, v248\n"
        "v_max3_f32 v248, v142, v143, v248\n"
        "v_max3_f32 v248, v144, v145, v248\n"
        "v_max3_f32 v248, v146, v147, v248\n"
        "v_max3_f32 v248, v148, v149, v248\n"
        "v_max3_f32 v248, v150, v151, v248\n"
        "s_nop 1\n"
        "s_branch .Lmk_slow3\n"
        ".Lmk_slow2:\n"
        "v_sub_f32 v136, v136, v248\n"
        "v_sub_f32 v137, v137, v248\n"
        "v_sub_f32 v138, v138, v248\n"
        "v_sub_f32 v139, v139, v248\n"
        "v_sub_f32 v140, v140, v248\n"
        "v_sub_f32 v141, v141, v248\n"
        "v_sub_f32 v142, v142, v248\n"
        "v_sub_f32 v143, v143, v248\n"
        "v_sub_f32 v144, v144, v248\n"
        "v_sub_f32 v145, v145, v248\n"
        "v_sub_f32 v146, v146, v248\n"
        "v_sub_f32 v147, v147, v248\n"
        "v_sub_f32 v148, v148, v248\n"
        "v_sub_f32 v149, v149, v248\n"
        "v_sub_f32 v150, v150, v248\n"
        "v_sub_f32 v151, v151, v248\n"
        "v_exp_f32 v136, v136\n"
        "v_exp_f32 v137, v137\n"
        "v_exp_f32 v138, v138\n"
        "v_exp_f32 v139, v139\n"
        "v_exp_f32 v140, v140\n"
        "v_exp_f32 v141, v141\n"
        "v_exp_f32 v142, v142\n"
        "v_exp_f32 v143, v143\n"
        "v_exp_f32 v144, v144\n"
        "v_exp_f32 v145, v145\n"
        "v_exp_f32 v146, v146\n"
        "v_exp_f32 v147, v147\n"
        "v_exp_f32 v148, v148\n"
        "v_exp_f32 v149, v149\n"
        "v_exp_f32 v150, v150\n"
        "v_exp_f32 v151, v151\n"
        "v_cvt_pk_bf16_f32 v184, v136, v137\n"
        "v_cvt_pk_bf16_f32 v185, v138, v139\n"
        "v_cvt_pk_bf16_f32 v186, v140, v141\n"
        "v_cvt_pk_bf16_f32 v187, v142, v143\n"
        "v_cvt_pk_bf16_f32 v188, v144, v145\n"
        "v_cvt_pk_bf16_f32 v189, v146, v147\n"
        "v_cvt_pk_bf16_f32 v190, v148, v149\n"
        "v_cvt_pk_bf16_f32 v191, v150, v151\n"
        "v_mfma_f32_32x32x16_bf16 v[168:183], v[208:211], v[184:187], v[168:183]\n"
        "s_nop 0\n"
        "v_mfma_f32_32x32x16_bf16 v[168:183], v[212:215], v[188:191], v[168:183]\n"
        "s_nop 11\n"
        ".Lmk_slow3:\n"
        "s_add_u32 s92, s92, 1\n"
        "s_cmp_le_u32 s92, 8\n"
        "s_cbranch_scc1 .Lmk_sl\n"
        "s_cmp_eq_u32 s93, 0\n"
        "s_cbranch_scc0 .Lmk_norm\n"
        "v_mov_b32 v249, v248\n"
        "s_nop 1\n"
        "v_permlane32_swap_b32 v249, v248\n"
        "v_max_f32 v248, v248, v249\n"
        "s_mov_b32 s93, 1\n"
        "s_nop 0\n"
        "s_branch .Lmk_sp\n"
        ".Lmk_router:\n"
        "v_lshl_add_u32 v249, s42, 6, v0\n"
        "v_subrev_u32 v248, 280, v249\n"
        "v_mov_b32 v250, 0x3f80\n"
        "v_mov_b32 v251, 0\n"
        "v_lshlrev_b32 v252, 3, v249\n"
        "v_ashrrev_i32 v248, 31, v248\n"
        "v_bfi_b32 v250, v248, 0, v250\n"
        "v_add_u32 v252, 155648, v252\n"
        "ds_write_b64 v252, v[250:251]\n"
        "v_and_b32 v254, 0x3f80, v248\n"
        "v_mov_b32 v255, 0\n"
        "v_min_u32 v252, 159480, v252\n"
        "ds_write_b64 v252, v[254:255] offset:4096\n"
        "s_mul_i32 s98, s43, 27\n"
        "s_add_u32 s98, s98, s44\n"
        "s_sub_u32 s98, s98, 1\n"
        "s_mul_i32 s99, s98, 171\n"
        "s_lshr_b32 s99, s99, 9\n"
        "s_mul_i32 s92, s99, 3\n"
        "s_sub_u32 s92, s98, s92\n"
        "s_mul_i32 s97, s92, 384\n"
        "s_lshl_b32 s96, s98, 10\n"
        "s_mul_i32 s92, s92, 12\n"
        "s_add_u32 s92, s92, s45\n"
        "s_mul_i32 s92, s92, 147456\n"
        "s_lshl_b32 s99, s99, 7\n"
        "s_add_u32 s54, s92, s99\n"
        "v_lshrrev_b32 v248, 3, v0\n"
        "v_and_b32 v249, 1, v14\n"
        "v_bfe_u32 v250, v0, 1, 2\n"
        "v_xor_b32 v249, v249, v250\n"
        "v_and_b32 v250, 1, v0\n"
        "v_lshl_or_b32 v249, v249, 1, v250\n"
        "v_mul_u32_u24 v250, 9216, v248\n"
        "s_sub_u32 s94, s44, 1\n"
        "s_cmp_ge_u32 s94, 27\n"
        "s_cselect_b32 s94, 2147483648, 0\n"
        "s_cselect_b32 s54, 0, s54\n"
        "s_cselect_b32 s97, 0, s97\n"
        "s_cselect_b32 s96, 2147483648, s96\n"
        "v_lshl_add_u32 v1, v249, 4, v250\n"
        "v_or_b32 v1, s94, v1\n"
        "v_xor_b32 v2, 64, v1\n"
        "v_add_u32 v2, 72704, v2\n"
        "s_lshr_b32 s98, s43, 1\n"
        "s_sub_u32 s99, s44, 28\n"
        "s_lshl2_add_u32 s98, s98, s99\n"
        "s_and_b32 s94, s43, 1\n"
        "s_lshl1_add_u32 s92, s98, s94\n"
        "s_lshl3_add_u32 s92, s92, s42\n"
        "s_or_b32 s98, s98, 1\n"
        "s_xor_b32 s94, s94, 1\n"
        "s_lshl1_add_u32 s98, s98, s94\n"
        "s_and_b32 s99, s99, 1\n"
        "s_lshl1_add_u32 s49, s98, s99\n"
        "v_lshl_add_u32 v249, s92, 3, v248\n"
        "v_and_b32 v250, 7, v0\n"
        "v_lshlrev_b32 v250, 4, v250\n"
        "v_mul_u32_u24 v251, 1152, v249\n"
        "v_add_u32 v251, v251, v250\n"
        "s_waitcnt lgkmcnt(0)\n"
        "s_load_dwordx2 s[40:41], s[12:13], 0x0\n"
        "s_cmp_eq_u32 s46, 0\n"
        "s_cbranch_scc0 .Lmk_s1\n"
        "s_bitcmp1_b32 s42, 1\n"
        "s_cselect_b64 s[88:89], s[38:39], s[36:37]\n"
        "s_and_b32 s94, s42, 1\n"
        "s_lshl_b32 s94, s94, 10\n"
        "v_lshl_add_u32 v254, v0, 4, s94\n"
        "v_min_u32 v254, 1136, v254\n"
        "s_lshl_b32 m0, s45, 10\n"
        "s_nop 0\n"
        "global_load_lds_dwordx4 v254, s[88:89]\n"
        ".Lmk_s1:\n"
        "buffer_load_dwordx4 v[16:19], v251, s[4:7], 0 offen offset:0\n"
        "buffer_load_dwordx4 v[20:23], v251, s[4:7], 0 offen offset:128\n"
        "buffer_load_dwordx4 v[24:27], v251, s[4:7], 0 offen offset:256\n"
        "buffer_load_dwordx4 v[28:31], v251, s[4:7], 0 offen offset:384\n"
        "buffer_load_dwordx4 v[32:35], v251, s[4:7], 0 offen offset:512\n"
        "buffer_load_dwordx4 v[36:39], v251, s[4:7], 0 offen offset:640\n"
        "buffer_load_dwordx4 v[40:43], v251, s[4:7], 0 offen offset:768\n"
        "buffer_load_dwordx4 v[44:47], v251, s[4:7], 0 offen offset:896\n"
        "buffer_load_dwordx4 v[48:51], v251, s[4:7], 0 offen offset:1024\n"
        "s_waitcnt vmcnt(9)\n"
        "s_barrier\n"
        "ds_read_b128 v[52:55], v250 offset:0\n"
        "ds_read_b128 v[88:91], v250 offset:2048\n"
        "ds_read_b128 v[56:59], v250 offset:128\n"
        "ds_read_b128 v[92:95], v250 offset:2176\n"
        "ds_read_b128 v[60:63], v250 offset:256\n"
        "ds_read_b128 v[96:99], v250 offset:2304\n"
        "ds_read_b128 v[64:67], v250 offset:384\n"
        "ds_read_b128 v[100:103], v250 offset:2432\n"
        "ds_read_b128 v[68:71], v250 offset:512\n"
        "ds_read_b128 v[104:107], v250 offset:2560\n"
        "ds_read_b128 v[72:75], v250 offset:640\n"
        "ds_read_b128 v[108:111], v250 offset:2688\n"
        "ds_read_b128 v[76:79], v250 offset:768\n"
        "ds_read_b128 v[112:115], v250 offset:2816\n"
        "ds_read_b128 v[80:83], v250 offset:896\n"
        "ds_read_b128 v[116:119], v250 offset:2944\n"
        "ds_read_b128 v[84:87], v250 offset:1024\n"
        "ds_read_b128 v[120:123], v250 offset:3072\n"
        "v_mov_b32 v124, 0\n"
        "v_mov_b32 v125, 0\n"
        "s_waitcnt lgkmcnt(0)\n"
        "s_mul_i32 s98, s40, 0x5851f42d\n"
        "s_mul_hi_u32 s99, s40, 0x4c957f2d\n"
        "s_mul_i32 s92, s41, 0x4c957f2d\n"
        "s_add_u32 s98, s98, s99\n"
        "s_add_u32 s98, s98, s92\n"
        "s_mul_i32 s40, s40, 0x4c957f2d\n"
        "s_add_u32 s40, s40, 0xf767814f\n"
        "s_addc_u32 s41, s98, 0x14057b7e\n"
        "s_waitcnt vmcnt(8)\n"
        "v_dot2c_f32_bf16 v124, v16, v52\n"
        "v_dot2c_f32_bf16 v125, v16, v88\n"
        "v_dot2c_f32_bf16 v124, v17, v53\n"
        "v_dot2c_f32_bf16 v125, v17, v89\n"
        "v_dot2c_f32_bf16 v124, v18, v54\n"
        "v_dot2c_f32_bf16 v125, v18, v90\n"
        "v_dot2c_f32_bf16 v124, v19, v55\n"
        "v_dot2c_f32_bf16 v125, v19, v91\n"
        "s_waitcnt vmcnt(7)\n"
        "v_dot2c_f32_bf16 v124, v20, v56\n"
        "v_dot2c_f32_bf16 v125, v20, v92\n"
        "v_dot2c_f32_bf16 v124, v21, v57\n"
        "v_dot2c_f32_bf16 v125, v21, v93\n"
        "v_dot2c_f32_bf16 v124, v22, v58\n"
        "v_dot2c_f32_bf16 v125, v22, v94\n"
        "v_dot2c_f32_bf16 v124, v23, v59\n"
        "v_dot2c_f32_bf16 v125, v23, v95\n"
        "s_waitcnt vmcnt(6)\n"
        "v_dot2c_f32_bf16 v124, v24, v60\n"
        "v_dot2c_f32_bf16 v125, v24, v96\n"
        "v_dot2c_f32_bf16 v124, v25, v61\n"
        "v_dot2c_f32_bf16 v125, v25, v97\n"
        "v_dot2c_f32_bf16 v124, v26, v62\n"
        "v_dot2c_f32_bf16 v125, v26, v98\n"
        "v_dot2c_f32_bf16 v124, v27, v63\n"
        "v_dot2c_f32_bf16 v125, v27, v99\n"
        "s_waitcnt vmcnt(5)\n"
        "v_dot2c_f32_bf16 v124, v28, v64\n"
        "v_dot2c_f32_bf16 v125, v28, v100\n"
        "v_dot2c_f32_bf16 v124, v29, v65\n"
        "v_dot2c_f32_bf16 v125, v29, v101\n"
        "v_dot2c_f32_bf16 v124, v30, v66\n"
        "v_dot2c_f32_bf16 v125, v30, v102\n"
        "v_dot2c_f32_bf16 v124, v31, v67\n"
        "v_dot2c_f32_bf16 v125, v31, v103\n"
        "s_waitcnt vmcnt(4)\n"
        "v_dot2c_f32_bf16 v124, v32, v68\n"
        "v_dot2c_f32_bf16 v125, v32, v104\n"
        "v_dot2c_f32_bf16 v124, v33, v69\n"
        "v_dot2c_f32_bf16 v125, v33, v105\n"
        "v_dot2c_f32_bf16 v124, v34, v70\n"
        "v_dot2c_f32_bf16 v125, v34, v106\n"
        "v_dot2c_f32_bf16 v124, v35, v71\n"
        "v_dot2c_f32_bf16 v125, v35, v107\n"
        "s_waitcnt vmcnt(3)\n"
        "v_dot2c_f32_bf16 v124, v36, v72\n"
        "v_dot2c_f32_bf16 v125, v36, v108\n"
        "v_dot2c_f32_bf16 v124, v37, v73\n"
        "v_dot2c_f32_bf16 v125, v37, v109\n"
        "v_dot2c_f32_bf16 v124, v38, v74\n"
        "v_dot2c_f32_bf16 v125, v38, v110\n"
        "v_dot2c_f32_bf16 v124, v39, v75\n"
        "v_dot2c_f32_bf16 v125, v39, v111\n"
        "s_waitcnt vmcnt(2)\n"
        "v_dot2c_f32_bf16 v124, v40, v76\n"
        "v_dot2c_f32_bf16 v125, v40, v112\n"
        "v_dot2c_f32_bf16 v124, v41, v77\n"
        "v_dot2c_f32_bf16 v125, v41, v113\n"
        "v_dot2c_f32_bf16 v124, v42, v78\n"
        "v_dot2c_f32_bf16 v125, v42, v114\n"
        "v_dot2c_f32_bf16 v124, v43, v79\n"
        "v_dot2c_f32_bf16 v125, v43, v115\n"
        "s_waitcnt vmcnt(1)\n"
        "v_dot2c_f32_bf16 v124, v44, v80\n"
        "v_dot2c_f32_bf16 v125, v44, v116\n"
        "v_dot2c_f32_bf16 v124, v45, v81\n"
        "v_dot2c_f32_bf16 v125, v45, v117\n"
        "v_dot2c_f32_bf16 v124, v46, v82\n"
        "v_dot2c_f32_bf16 v125, v46, v118\n"
        "v_dot2c_f32_bf16 v124, v47, v83\n"
        "v_dot2c_f32_bf16 v125, v47, v119\n"
        "s_waitcnt vmcnt(0)\n"
        "v_dot2c_f32_bf16 v124, v48, v84\n"
        "v_dot2c_f32_bf16 v125, v48, v120\n"
        "v_dot2c_f32_bf16 v124, v49, v85\n"
        "v_dot2c_f32_bf16 v125, v49, v121\n"
        "v_dot2c_f32_bf16 v124, v50, v86\n"
        "v_dot2c_f32_bf16 v125, v50, v122\n"
        "v_dot2c_f32_bf16 v124, v51, v87\n"
        "v_dot2c_f32_bf16 v125, v51, v123\n"
        "s_nop 1\n"
        "v_add_f32_dpp v124, v124, v124 quad_perm:[1,0,3,2] row_mask:0xf bank_mask:0xf\n"
        "v_add_f32_dpp v125, v125, v125 quad_perm:[1,0,3,2] row_mask:0xf bank_mask:0xf\n"
        "s_nop 0\n"
        "v_add_f32_dpp v124, v124, v124 quad_perm:[2,3,0,1] row_mask:0xf bank_mask:0xf\n"
        "v_add_f32_dpp v125, v125, v125 quad_perm:[2,3,0,1] row_mask:0xf bank_mask:0xf\n"
        "s_nop 0\n"
        "v_add_f32_dpp v124, v124, v124 row_half_mirror row_mask:0xf bank_mask:0xf\n"
        "v_add_f32_dpp v125, v125, v125 row_half_mirror row_mask:0xf bank_mask:0xf\n"
        "v_mul_f32 v126, 0xbfb8aa3b, v125\n"
        "v_exp_f32 v126, v126\n"
        "s_nop 0\n"
        "v_add_f32 v126, 1.0, v126\n"
        "v_rcp_f32 v126, v126\n"
        "v_mov_b32 v127, s40\n"
        "v_lshlrev_b32 v250, 3, v249\n"
        "v_and_b32 v251, 0x7ff, v124\n"
        "v_add_u32 v251, 0x7ff, v251\n"
        "v_and_b32 v251, 0x800, v251\n"
        "v_or_b32 v251, v251, v124\n"
        "v_ashrrev_i32 v252, 31, v251\n"
        "v_or_b32 v252, 0x80000000, v252\n"
        "v_xor_b32 v251, v251, v252\n"
        "v_and_b32 v251, 0xfffff800, v251\n"
        "v_sub_u32 v252, 0x7ff, v249\n"
        "v_or_b32 v251, v251, v252\n"
        "v_mov_b32 v252, 155664\n"
        "s_mov_b32 s93, 16384\n"
        "s_mov_b32 exec_lo, 0x01010101\n"
        "s_mov_b32 exec_hi, 0x01010101\n"
        "ds_max_u32 v252, v251\n"
        "buffer_store_dwordx2 v[126:127], v250, s[12:15], s93 offen sc1\n"
        "s_mov_b64 exec, -1\n"
        "s_waitcnt lgkmcnt(0)\n"
        "s_barrier\n"
        "s_cmp_eq_u32 s42, 0\n"
        "s_cbranch_scc0 .Lmk_s2\n"
        "ds_read_b32 v126, v252\n"
        "s_sub_u32 s98, s44, 28\n"
        "s_lshl4_add_u32 s98, s98, s43\n"
        "s_lshl3_add_u32 s98, s98, 12320768\n"
        "v_mul_u32_u24 v251, 4352, v0\n"
        "s_mov_b64 exec, 0xffff\n"
        "s_waitcnt lgkmcnt(0)\n"
        "buffer_store_dwordx2 v[126:127], v251, s[12:15], s98 offen sc1\n"
        "s_mov_b64 exec, -1\n"
        ".Lmk_s2:\n"
        "v_mov_b32 v253, 2147483648\n"
        "s_sub_u32 s92, s2, 224\n"
        "s_movk_i32 s98, -162\n"
        "s_movk_i32 s99, -18\n"
        "s_mul_i32 s93, s92, 20736\n"
        "s_mul_i32 s92, s92, 2592\n"
        "s_add_u32 s92, s92, 8388608\n"
        "s_movk_i32 s94, 2304\n"
        "v_lshl_add_u32 v43, s42, 6, v0\n"
        "v_mov_b32 v44, 992\n"
        "v_mov_b32 v45, 80\n"
        "v_add_u32 v40, 0, v43\n"
        "v_mul_u32_u24 v250, 6473, v40\n"
        "v_lshrrev_b32 v250, 20, v250\n"
        "v_mad_i32_i24 v40, v250, s98, v40\n"
        "v_mul_u32_u24 v251, 3641, v40\n"
        "v_lshrrev_b32 v251, 16, v251\n"
        "v_mad_i32_i24 v252, v251, s99, v40\n"
        "v_cmp_lt_u32 vcc, 9, v252\n"
        "s_nop 1\n"
        "v_cndmask_b32_e32 v254, 0, v44, vcc\n"
        "v_cmp_eq_u32 vcc, 9, v252\n"
        "v_mad_u32_u24 v251, v251, s94, v254\n"
        "s_nop 0\n"
        "v_cndmask_b32_e32 v252, v252, v45, vcc\n"
        "v_lshl_add_u32 v251, v252, 4, v251\n"
        "v_mul_u32_u24 v252, 144, v250\n"
        "v_add_u32 v251, v251, v252\n"
        "buffer_load_dwordx4 v[24:27], v251, s[28:31], s93 offen\n"
        "v_mul_u32_u24 v250, 82944, v250\n"
        "v_lshl_add_u32 v40, v40, 4, v250\n"
        "v_add_u32 v41, 512, v43\n"
        "v_mul_u32_u24 v250, 6473, v41\n"
        "v_lshrrev_b32 v250, 20, v250\n"
        "v_mad_i32_i24 v41, v250, s98, v41\n"
        "v_mul_u32_u24 v251, 3641, v41\n"
        "v_lshrrev_b32 v251, 16, v251\n"
        "v_mad_i32_i24 v252, v251, s99, v41\n"
        "v_cmp_lt_u32 vcc, 9, v252\n"
        "s_nop 1\n"
        "v_cndmask_b32_e32 v254, 0, v44, vcc\n"
        "v_cmp_eq_u32 vcc, 9, v252\n"
        "v_mad_u32_u24 v251, v251, s94, v254\n"
        "s_nop 0\n"
        "v_cndmask_b32_e32 v252, v252, v45, vcc\n"
        "v_lshl_add_u32 v251, v252, 4, v251\n"
        "v_mul_u32_u24 v252, 144, v250\n"
        "v_add_u32 v251, v251, v252\n"
        "buffer_load_dwordx4 v[28:31], v251, s[28:31], s93 offen\n"
        "v_mul_u32_u24 v250, 82944, v250\n"
        "v_lshl_add_u32 v41, v41, 4, v250\n"
        "v_add_u32 v42, 1024, v43\n"
        "v_min_u32 v42, 1295, v42\n"
        "v_mul_u32_u24 v250, 6473, v42\n"
        "v_lshrrev_b32 v250, 20, v250\n"
        "v_mad_i32_i24 v42, v250, s98, v42\n"
        "v_mul_u32_u24 v251, 3641, v42\n"
        "v_lshrrev_b32 v251, 16, v251\n"
        "v_mad_i32_i24 v252, v251, s99, v42\n"
        "v_cmp_lt_u32 vcc, 9, v252\n"
        "s_nop 1\n"
        "v_cndmask_b32_e32 v254, 0, v44, vcc\n"
        "v_cmp_eq_u32 vcc, 9, v252\n"
        "v_mad_u32_u24 v251, v251, s94, v254\n"
        "s_nop 0\n"
        "v_cndmask_b32_e32 v252, v252, v45, vcc\n"
        "v_lshl_add_u32 v251, v252, 4, v251\n"
        "v_mul_u32_u24 v252, 144, v250\n"
        "v_add_u32 v251, v251, v252\n"
        "buffer_load_dwordx4 v[32:35], v251, s[28:31], s93 offen\n"
        "v_mul_u32_u24 v250, 82944, v250\n"
        "v_lshl_add_u32 v42, v42, 4, v250\n"
        "v_cmp_gt_u32 vcc, 272, v43\n"
        "s_nop 1\n"
        "v_cndmask_b32_e32 v42, v253, v42, vcc\n"
        "s_waitcnt vmcnt(2)\n"
        "buffer_store_dwordx4 v[24:27], v40, s[12:15], s92 offen sc1\n"
        "s_waitcnt vmcnt(2)\n"
        "buffer_store_dwordx4 v[28:31], v41, s[12:15], s92 offen sc1\n"
        "s_waitcnt vmcnt(2)\n"
        "buffer_store_dwordx4 v[32:35], v42, s[12:15], s92 offen sc1\n"
        "s_waitcnt vmcnt(0)\n"
        "s_barrier\n"
        "s_cmp_eq_u32 s42, 0\n"
        "s_cbranch_scc0 .Lmk_s3\n"
        "s_sub_u32 s98, s44, 28\n"
        "s_lshl4_add_u32 s98, s98, s43\n"
        "s_lshl3_add_u32 s98, s98, 12321280\n"
        "v_mul_u32_u24 v251, 4352, v0\n"
        "s_mov_b64 exec, 0xffff\n"
        "buffer_store_dwordx2 v[126:127], v251, s[12:15], s98 offen sc1\n"
        "s_mov_b64 exec, -1\n"
        ".Lmk_s3:\n"
        "v_mul_u32_u24 v252, 57, v0\n"
        "v_lshrrev_b32 v252, 9, v252\n"
        "v_mul_u32_u24 v251, 9, v252\n"
        "v_sub_u32 v251, v0, v251\n"
        "s_sub_u32 s92, s44, 28\n"
        "s_lshl3_add_u32 s92, s92, s42\n"
        "s_mul_i32 s92, s92, 7\n"
        "v_min_u32 v252, 6, v252\n"
        "v_add_u32 v252, s92, v252\n"
        "v_lshrrev_b32 v250, 6, v252\n"
        "v_lshl_add_u32 v252, v250, 7, v252\n"
        "v_mul_u32_u24 v252, 9216, v252\n"
        "v_lshl_add_u32 v252, v251, 7, v252\n"
        "s_mul_i32 s92, s43, 1152\n"
        "s_add_u32 s93, s92, 1179648\n"
        "buffer_load_dword v65, v252, s[20:23], s93 offen\n"
        "s_cmp_eq_u32 s46, 0\n"
        "s_cbranch_scc0 .Lmk_s4\n"
        "v_and_b32 v12, 31, v0\n"
        "v_and_b32 v13, 24, v0\n"
        "v_add_u32 v12, v12, v13\n"
        "s_and_b32 s93, s2, 15\n"
        "s_mul_i32 s93, s93, 4352\n"
        "v_lshlrev_b32 v12, 3, v12\n"
        "s_add_u32 s93, s93, 12320768\n"
        "buffer_load_dwordx2 v[12:13], v12, s[12:15], s93 offen sc1\n"
        "s_waitcnt vmcnt(0)\n"
        ".Lmk_s4:\n"
        "s_mov_b32 s56, 0x40000000\n"
        "s_branch .Lmk_join\n"
        ".Lmk_last:\n"
        : "+{s[0:1]}"(ka), "+{s2}"(bid), "+{v0}"(tid)
        :
        : "memory", "vcc", "scc", "s3", "s4", "s5", "s6", "s7", "s8", "s9", "s10", "s11", "s12", "s13", "s14", "s15", "s16", "s17", "s18", "s19", "s20", "s21", "s22", "s23", "s24", "s25", "s26", "s27", "s28", "s29", "s30", "s31", "s33", "s34", "s35", "s36", "s37", "s38", "s39", "s40", "s41", "s42", "s43", "s44", "s45", "s46", "s47", "s48", "s49", "s50", "s51", "s52", "s53", "s54", "s55", "s56", "s57", "s58", "s59", "s60", "s61", "s62", "s63", "s64", "s65", "s66", "s67", "s68", "s69", "s70", "s71", "s72", "s73", "s74", "s75", "s76", "s77", "s78", "s79", "s80", "s81", "s82", "s83", "s84", "s85", "s86", "s87", "s88", "s89", "s90", "s91", "s92", "s93", "s94", "s95", "s96", "s97", "s98", "s99", "s100", "v1", "v2", "v3", "v4", "v5", "v6", "v7", "v8", "v9", "v10", "v11", "v12", "v13", "v14", "v15", "v16", "v17", "v18", "v19", "v20", "v21", "v22", "v23", "v24", "v25", "v26", "v27", "v28", "v29", "v30", "v31", "v32", "v33", "v34", "v35", "v36", "v37", "v38", "v39", "v40", "v41", "v42", "v43", "v44", "v45", "v46", "v47", "v48", "v49", "v50", "v51", "v52", "v53", "v54", "v55", "v56", "v57", "v58", "v59", "v60", "v61", "v62", "v63", "v64", "v65", "v66", "v67", "v68", "v69", "v70", "v71", "v72", "v73", "v74", "v75", "v76", "v77", "v78", "v79", "v80", "v81", "v82", "v83", "v84", "v85", "v86", "v87", "v88", "v89", "v90", "v91", "v92", "v93", "v94", "v95", "v96", "v97", "v98", "v99", "v100", "v101", "v102", "v103", "v104", "v105", "v106", "v107", "v108", "v109", "v110", "v111", "v112", "v113", "v114", "v115", "v116", "v117", "v118", "v119", "v120", "v121", "v122", "v123", "v124", "v125", "v126", "v127", "v128", "v129", "v130", "v131", "v132", "v133", "v134", "v135", "v136", "v137", "v138", "v139", "v140", "v141", "v142", "v143", "v144", "v145", "v146", "v147", "v148", "v149", "v150", "v151", "v152", "v153", "v154", "v155", "v156", "v157", "v158", "v159", "v160", "v161", "v162", "v163", "v164", "v165", "v166", "v167", "v168", "v169", "v170", "v171", "v172", "v173", "v174", "v175", "v176", "v177", "v178", "v179", "v180", "v181", "v182", "v183", "v184", "v185", "v186", "v187", "v188", "v189", "v190", "v191", "v192", "v193", "v194", "v195", "v196", "v197", "v198", "v199", "v200", "v201", "v202", "v203", "v204", "v205", "v206", "v207", "v208", "v209", "v210", "v211", "v212", "v213", "v214", "v215", "v216", "v217", "v218", "v219", "v220", "v221", "v222", "v223", "v224", "v225", "v226", "v227", "v228", "v229", "v230", "v231", "v232", "v233", "v234", "v235", "v236", "v237", "v238", "v239", "v240", "v241", "v242", "v243", "v244", "v245", "v246", "v247", "v248", "v249", "v250", "v251", "v252", "v253", "v254", "v255");
}

extern "C" void kernel_launch(void* const* d_in, const int* in_sizes, int n_in, void* d_out, int out_size, void* d_ws, size_t ws_size,
                              hipStream_t stream) {
    const u32 F = 0x00020000u;
    (void)hipFuncSetAttribute((const void*)fused_kernel, hipFuncAttributeMaxDynamicSharedMemorySize, 163840);
    fused_kernel<<<256, 512, 163840, stream>>>(d_in[0], 2048u * 1152u, F, d_in[1], 576u * 3456u, F, d_ws, 12582912u, F, d_in[5], 576u * 9216u, F,
                                               d_in[6], 576u * 9216u, F, d_in[7], 4608u * 1152u, F, d_in[2], 576u * 1152u, F, d_out,
                                               2048u * 1152u, F, d_in[4], d_in[8]);
}
```
